```python
import torch

def _softmax(x, axis=-1):
    return torch.softmax(x, dim=axis)
def _gelu(x):
    return torch.nn.functional.gelu(x, approximate="tanh")
def _silu(x):
    return torch.nn.functional.silu(x)
def _sigmoid(x):
    return torch.sigmoid(x)
def _layernorm(x, axis=-1, eps=1e-5):
    m = x.mean(dim=axis, keepdim=True)
    v = x.var(dim=axis, keepdim=True, unbiased=False)
    return (x - m) / torch.sqrt(v + eps)
def _scatter_add(tgt, idx, src, axis=0):
    return tgt.clone().index_add_(axis, idx, src)
def _segment_sum(src, seg, n):
    out = torch.zeros((n,) + src.shape[1:], dtype=src.dtype)
    return out.index_add_(0, seg, src)
def _pad(x, pw):
    flat = tuple(v for pair in reversed(pw) for v in pair)
    return torch.nn.functional.pad(x, flat)

kernel_name = 'interleave_layers__linear_kernel_hedgehog__softmax_full_mha__prompt'

def setup_inputs():

    _w = (lambda a: (lambda t: t.to(torch.bfloat16) if t.is_floating_point() else t)(torch.as_tensor(a)))
    def _A_setup():
        B, S, D, H = 1, 2048, 576, 32
        Dh = D // H
        return {
            "x": _w((lambda *s: torch.randn(*s, dtype=torch.bfloat16) * 0.05)(B, S, D)),
            "wqkv": _w((lambda *s: torch.randn(*s, dtype=torch.bfloat16) * 0.4)(D, 3 * D)),
            "w_phi": _w((lambda *s: torch.randn(*s, dtype=torch.bfloat16) * 0.4)(H, Dh, Dh) * (Dh ** -0.5)),
            "wo": _w((lambda *s: torch.randn(*s, dtype=torch.bfloat16) * 0.05)(D, D)),
        }
    _A_in = _A_setup()

    def _B_setup():
        B, S, D = 1, 2048, 576
        return {
            "x": _w((lambda *s: torch.randn(*s, dtype=torch.bfloat16) * 0.05)(B, S, D)),
            "wqkv": _w((lambda *s: torch.randn(*s, dtype=torch.bfloat16) * 0.1)(D, 3 * D)),
            "wo": _w((lambda *s: torch.randn(*s, dtype=torch.bfloat16) * 0.05)(D, D)),
        }
    _B_in = _B_setup()

    return {
        "A_x": _A_in["x"],
        "A_wqkv": _A_in["wqkv"],
        "A_w_phi": _A_in["w_phi"],
        "A_wo": _A_in["wo"],
        "B_x": _B_in["x"],
        "B_wqkv": _B_in["wqkv"],
        "B_wo": _B_in["wo"],
    }

def reference(A_x, A_wqkv, A_w_phi, A_wo, B_x, B_wqkv, B_wo):
    def _A_ref(x, wqkv, w_phi, wo):
        B, S, D = x.shape
        H = 32; Dh = D // H
        qkv = (lambda x, *d: x.permute(*d))((x @ wqkv).reshape(B, S, 3, H, Dh), 2, 0, 3, 1, 4)
        q, k, v = qkv[0], qkv[1], qkv[2]
        pq = torch.einsum("bhsd,hdf->bhsf", q, w_phi)
        pk = torch.einsum("bhsd,hdf->bhsf", k, w_phi)
        phi_q = torch.concatenate([_softmax(pq, axis=-1), _softmax(-pq, axis=-1)], axis=-1)
        phi_k = torch.concatenate([_softmax(pk, axis=-1), _softmax(-pk, axis=-1)], axis=-1)
        kv = torch.einsum("bhsf,bhsd->bhfd", phi_k, v)
        z  = torch.einsum("bhsf->bhf", phi_k)
        num = torch.einsum("bhqf,bhfd->bhqd", phi_q, kv)
        den = torch.einsum("bhqf,bhf->bhq", phi_q, z)[..., None] + 1e-6
        o = num / den
        return (lambda x, *d: x.permute(*d))(o, 0, 2, 1, 3).reshape(B, S, D) @ wo

    def _B_ref(x, wqkv, wo):
        B, S, D = x.shape
        H = 32; Dh = D // H
        qkv = (lambda x, *d: x.permute(*d))((x @ wqkv).reshape(B, S, 3, H, Dh), 2, 0, 3, 1, 4)
        q, k, v = qkv[0], qkv[1], qkv[2]
        scale = Dh ** -0.5
        scores = torch.einsum("bhqd,bhkd->bhqk", q, k) * scale
        p = _softmax(scores, axis=-1)
        o = torch.einsum("bhqk,bhkd->bhqd", p, v)
        return (lambda x, *d: x.permute(*d))(o, 0, 2, 1, 3).reshape(B, S, D) @ wo

    x = A_x
    x = x + _A_ref(_layernorm(x, axis=-1), A_wqkv, A_w_phi, A_wo)
    x = x + _B_ref(_layernorm(x, axis=-1), B_wqkv, B_wo)
    x = x + _A_ref(_layernorm(x, axis=-1), A_wqkv, A_w_phi, A_wo)
    x = x + _B_ref(_layernorm(x, axis=-1), B_wqkv, B_wo)
    return x
```

```cpp
#include <hip/hip_runtime.h>
#include <hip/hip_bf16.h>
#include <stdint.h>

typedef __attribute__((ext_vector_type(8))) short bf16x8;
typedef __attribute__((ext_vector_type(4))) float f32x4;
typedef unsigned short u16;
typedef unsigned int u32;

#define S_LEN 2048
#define DM 576
#define NH 32
#define DH 18
#define N3 1728
#define PQ_STRIDE 1280

static __device__ __forceinline__ float bf2f(u16 v) { return __uint_as_float(((u32)v) << 16); }
static __device__ __forceinline__ u16 f2bf(float f) {
  u32 u = __float_as_uint(f);
  u += 0x7FFF + ((u >> 16) & 1);
  return (u16)(u >> 16);
}
static __device__ __forceinline__ u32 cvtpk(float lo, float hi) {
  u32 r;
  asm("v_cvt_pk_bf16_f32 %0, %1, %2" : "=v"(r) : "v"(lo), "v"(hi));
  return r;
}
#define MFMA16(a, b, c) __builtin_amdgcn_mfma_f32_16x16x32_bf16(a, b, c, 0, 0, 0)

__global__ __launch_bounds__(256) void prep_kernel(
    const u16* __restrict__ wqkvA, const u16* __restrict__ wphi, const u16* __restrict__ woA,
    const u16* __restrict__ wqkvB, const u16* __restrict__ woB,
    u16* __restrict__ WtA, float* __restrict__ csA, u16* __restrict__ WtB, float* __restrict__ csB,
    u16* __restrict__ woAt, u16* __restrict__ woBt, float* __restrict__ kvacc) {
  __shared__ float slab[DM * DH];
  __shared__ float wp[DH * DH];
  __shared__ float cs[DH];
  __shared__ u16 tile[64][66];
  const int b = blockIdx.x, tid = threadIdx.x;
  if (b < 192) {
    const bool isA = b < 96;
    const int bb = isA ? b : b - 96;
    const int t = bb / NH, h = bb % NH;
    const u16* W = isA ? wqkvA : wqkvB;
    for (int i = tid; i < DM * DH; i += 256) {
      int k = i / DH, d = i % DH;
      slab[i] = bf2f(W[(size_t)k * N3 + t * DM + h * DH + d]);
    }
    for (int i = tid; i < DH * DH; i += 256) wp[i] = bf2f(wphi[h * DH * DH + i]);
    if (tid < DH) cs[tid] = 0.f;
    __syncthreads();
    int nbase;
    if (isA) {
      if (t == 0) nbase = h * DH;
      else nbase = DM + (h / 4) * 144 + (t == 2 ? 72 : 0) + (h % 4) * DH;
    } else {
      nbase = t * DM + h * DH;
    }
    u16* Wt = isA ? WtA : WtB;
    const bool fold = isA && t < 2;
    float part[DH];
#pragma unroll
    for (int f = 0; f < DH; ++f) part[f] = 0.f;
    for (int k = tid; k < DM; k += 256) {
#pragma unroll
      for (int f = 0; f < DH; ++f) {
        float v;
        if (fold) {
          v = 0.f;
#pragma unroll
          for (int d = 0; d < DH; ++d) v += slab[k * DH + d] * wp[d * DH + f];
        } else {
          v = slab[k * DH + f];
        }
        u16 r = f2bf(v);
        Wt[(size_t)(nbase + f) * DM + k] = r;
        part[f] += bf2f(r);
      }
    }
#pragma unroll
    for (int f = 0; f < DH; ++f) atomicAdd(&cs[f], part[f]);
    __syncthreads();
    if (tid < DH) (isA ? csA : csB)[nbase + tid] = cs[tid];
  } else if (b < 354) {
    const bool isA = b < 273;
    const int bb = isA ? b - 192 : b - 273;
    const int tr = bb / 9, tc = bb % 9;
    const u16* W = isA ? woA : woB;
    u16* Wt = isA ? woAt : woBt;
    for (int i = tid; i < 4096; i += 256) {
      int r = i / 64, c = i % 64;
      tile[r][c] = W[(size_t)(tr * 64 + r) * DM + tc * 64 + c];
    }
    __syncthreads();
    for (int i = tid; i < 4096; i += 256) {
      int r = i / 64, c = i % 64;
      Wt[(size_t)(tc * 64 + r) * DM + tr * 64 + c] = tile[c][r];
    }
  } else {
    for (int i = tid; i < 2 * NH * 19 * 36; i += 256) kvacc[i] = 0.f;
  }
}

#define BSTR 80
#define TSTR 152

template <bool IS_A>
__global__ __launch_bounds__(256) void qkv_kernel(
    const u16* __restrict__ x, const u16* __restrict__ Wt, const float* __restrict__ colsum,
    u16* __restrict__ phiQ, float* __restrict__ kvacc,
    u16* __restrict__ qB, u16* __restrict__ kB, u16* __restrict__ vT) {
  __shared__ __attribute__((aligned(16))) unsigned char smem[126976];
  u16* Bs = (u16*)smem;
  const int tid = threadIdx.x, lane = tid & 63, w = tid >> 6;
  const int mt = blockIdx.x, nt = blockIdx.y;
  const int row0 = mt * 128 + w * 32;
  const int lr = lane & 15, lg = lane >> 4;

  f32x4 acc[2][9];
#pragma unroll
  for (int i = 0; i < 2; ++i)
#pragma unroll
    for (int j = 0; j < 9; ++j) acc[i][j] = (f32x4){0.f, 0.f, 0.f, 0.f};
  float s1[2] = {0.f, 0.f}, s2[2] = {0.f, 0.f};

  const u16* xa0 = x + (size_t)(row0 + lr) * DM + lg * 8;
  const u16* xa1 = xa0 + 16 * DM;
  const u16* Wg = Wt + (size_t)nt * 144 * DM;

  auto load_stage = [&](int st, int buf) {
#pragma unroll
    for (int i = 0; i < 5; ++i) {
      int c = tid + i * 256;
      if (c < 1152) {
        int r = c >> 3, kc = c & 7;
        bf16x8 v = *(const bf16x8*)(Wg + (size_t)r * DM + st * 64 + kc * 8);
        *(bf16x8*)(Bs + buf * 144 * BSTR + r * BSTR + kc * 8) = v;
      }
    }
  };
  load_stage(0, 0);
  __syncthreads();
  for (int st = 0; st < 9; ++st) {
    const int buf = st & 1;
    bf16x8 a[2][2];
    a[0][0] = *(const bf16x8*)(xa0 + st * 64);
    a[0][1] = *(const bf16x8*)(xa0 + st * 64 + 32);
    a[1][0] = *(const bf16x8*)(xa1 + st * 64);
    a[1][1] = *(const bf16x8*)(xa1 + st * 64 + 32);
    if (st + 1 < 9) load_stage(st + 1, buf ^ 1);
#pragma unroll
    for (int i = 0; i < 2; ++i)
#pragma unroll
      for (int s = 0; s < 2; ++s)
#pragma unroll
        for (int e = 0; e < 8; ++e) {
          float f = bf2f((u16)a[i][s][e]);
          s1[i] += f;
          s2[i] += f * f;
        }
#pragma unroll
    for (int s = 0; s < 2; ++s) {
#pragma unroll
      for (int j = 0; j < 9; ++j) {
        bf16x8 b = *(const bf16x8*)(Bs + buf * 144 * BSTR + (j * 16 + lr) * BSTR + s * 32 + lg * 8);
        acc[0][j] = MFMA16(a[0][s], b, acc[0][j]);
        acc[1][j] = MFMA16(a[1][s], b, acc[1][j]);
      }
    }
    __syncthreads();
  }
  float mean[2], rstd[2];
#pragma unroll
  for (int i = 0; i < 2; ++i) {
    float a1 = s1[i], a2 = s2[i];
    a1 += __shfl_xor(a1, 16); a1 += __shfl_xor(a1, 32);
    a2 += __shfl_xor(a2, 16); a2 += __shfl_xor(a2, 32);
    float m = a1 * (1.f / DM);
    float v = a2 * (1.f / DM) - m * m;
    v = fmaxf(v, 0.f);
    mean[i] = m;
    rstd[i] = rsqrtf(v + 1e-5f);
  }
  u16* T = (u16*)smem;
#pragma unroll
  for (int i = 0; i < 2; ++i) {
#pragma unroll
    for (int r = 0; r < 4; ++r) {
      int rr = lg * 4 + r;
      float m = __shfl(mean[i], rr), rs = __shfl(rstd[i], rr);
#pragma unroll
      for (int j = 0; j < 9; ++j) {
        int col = j * 16 + lr;
        float v = (acc[i][j][r] - m * colsum[nt * 144 + col]) * rs;
        T[(w * 32 + i * 16 + rr) * TSTR + col] = f2bf(v);
      }
    }
  }
  __syncthreads();
  if (IS_A) {
    if (nt < 4) {
      for (int task = tid; task < 128 * 8; task += 256) {
        int r = task >> 3, hh = task & 7;
        const u16* src = T + r * TSTR + hh * DH;
        float v[DH];
        float mx = -INFINITY, mn = INFINITY;
#pragma unroll
        for (int d = 0; d < DH; ++d) { v[d] = bf2f(src[d]); mx = fmaxf(mx, v[d]); mn = fminf(mn, v[d]); }
        float ep[DH], en[DH], sp = 0.f, sn = 0.f;
#pragma unroll
        for (int d = 0; d < DH; ++d) {
          ep[d] = __expf(v[d] - mx); sp += ep[d];
          en[d] = __expf(mn - v[d]); sn += en[d];
        }
        float ip = 1.f / sp, in_ = 1.f / sn;
        u32 o[20];
#pragma unroll
        for (int d = 0; d < 9; ++d) o[d] = cvtpk(ep[2 * d] * ip, ep[2 * d + 1] * ip);
#pragma unroll
        for (int d = 0; d < 9; ++d) o[9 + d] = cvtpk(en[2 * d] * in_, en[2 * d + 1] * in_);
        o[18] = 0; o[19] = 0;
        u32* dst = (u32*)(phiQ + (size_t)(mt * 128 + r) * PQ_STRIDE + (nt * 8 + hh) * 40);
#pragma unroll
        for (int q = 0; q < 5; ++q)
          *(uint4*)(dst + q * 4) = make_uint4(o[q * 4], o[q * 4 + 1], o[q * 4 + 2], o[q * 4 + 3]);
      }
    } else {
      u16* PK = (u16*)(smem + 128 * TSTR * 2);
      u16* VTs = PK + 4 * 48 * 136;
      for (int i = tid; i < 4 * 12 * 128; i += 256) {
        int hh = i / (12 * 128), rem = i % (12 * 128);
        PK[(hh * 48 + 36 + rem / 128) * 136 + rem % 128] = 0;
      }
      for (int i = tid; i < 4 * 14 * 128; i += 256) {
        int hh = i / (14 * 128), rem = i % (14 * 128);
        int d = 18 + rem / 128;
        VTs[(hh * 32 + d) * 136 + rem % 128] = (d == 18) ? (u16)0x3F80 : (u16)0;
      }
      for (int task = tid; task < 128 * 4; task += 256) {
        int r = task >> 2, hh = task & 3;
        const u16* src = T + r * TSTR + hh * DH;
        float v[DH];
        float mx = -INFINITY, mn = INFINITY;
#pragma unroll
        for (int d = 0; d < DH; ++d) { v[d] = bf2f(src[d]); mx = fmaxf(mx, v[d]); mn = fminf(mn, v[d]); }
        float ep[DH], en[DH], sp = 0.f, sn = 0.f;
#pragma unroll
        for (int d = 0; d < DH; ++d) {
          ep[d] = __expf(v[d] - mx); sp += ep[d];
          en[d] = __expf(mn - v[d]); sn += en[d];
        }
        float ip = 1.f / sp, in_ = 1.f / sn;
#pragma unroll
        for (int d = 0; d < DH; ++d) {
          PK[(hh * 48 + d) * 136 + r] = f2bf(ep[d] * ip);
          PK[(hh * 48 + 18 + d) * 136 + r] = f2bf(en[d] * in_);
          VTs[(hh * 32 + d) * 136 + r] = src[72 + d];
        }
      }
      __syncthreads();
      const int hh = w;
      f32x4 c[3][2];
#pragma unroll
      for (int i = 0; i < 3; ++i)
#pragma unroll
        for (int j = 0; j < 2; ++j) c[i][j] = (f32x4){0.f, 0.f, 0.f, 0.f};
#pragma unroll
      for (int s = 0; s < 4; ++s) {
        bf16x8 bb[2];
#pragma unroll
        for (int j = 0; j < 2; ++j) bb[j] = *(const bf16x8*)(VTs + (hh * 32 + j * 16 + lr) * 136 + s * 32 + lg * 8);
#pragma unroll
        for (int i = 0; i < 3; ++i) {
          bf16x8 aa = *(const bf16x8*)(PK + (hh * 48 + i * 16 + lr) * 136 + s * 32 + lg * 8);
#pragma unroll
          for (int j = 0; j < 2; ++j) c[i][j] = MFMA16(aa, bb[j], c[i][j]);
        }
      }
      const int h = (nt - 4) * 4 + hh;
#pragma unroll
      for (int i = 0; i < 3; ++i)
#pragma unroll
        for (int j = 0; j < 2; ++j)
#pragma unroll
          for (int r = 0; r < 4; ++r) {
            int f = i * 16 + lg * 4 + r, d = j * 16 + lr;
            if (f < 36 && d < 19) atomicAdd(&kvacc[(h * 19 + d) * 36 + f], c[i][j][r]);
          }
    }
  } else {
    const int t = nt / 4, hb = (nt % 4) * 8;
    if (t < 2) {
      const float sc = (t == 0) ? 0.23570226039551584f * 1.4426950408889634f : 1.f;
      u16* dstb = (t == 0) ? qB : kB;
      for (int task = tid; task < 128 * 8; task += 256) {
        int r = task >> 3, hh = task & 7;
        const u16* src = T + r * TSTR + hh * DH;
        u32 o[16];
#pragma unroll
        for (int d = 0; d < 9; ++d) o[d] = cvtpk(bf2f(src[2 * d]) * sc, bf2f(src[2 * d + 1]) * sc);
#pragma unroll
        for (int d = 9; d < 16; ++d) o[d] = 0;
        u32* dst = (u32*)(dstb + ((size_t)(hb + hh) * S_LEN + mt * 128 + r) * 32);
#pragma unroll
        for (int q = 0; q < 4; ++q)
          *(uint4*)(dst + q * 4) = make_uint4(o[q * 4], o[q * 4 + 1], o[q * 4 + 2], o[q * 4 + 3]);
      }
    } else {
      for (int i = tid; i < 144 * 128; i += 256) {
        int col = i >> 7, r = i & 127;
        vT[((size_t)(hb * DH + col)) * S_LEN + mt * 128 + r] = T[r * TSTR + col];
      }
    }
  }
}

#define OSTR 584
template <bool IS_A>
__global__ __launch_bounds__(256) void out_kernel(
    const u16* __restrict__ phiQ, const float* __restrict__ kvacc, const u16* __restrict__ oB,
    const u16* __restrict__ Wot, const u16* __restrict__ xin, u16* __restrict__ xout) {
  __shared__ __attribute__((aligned(16))) u16 O[32 * OSTR];
  __shared__ __attribute__((aligned(16))) u16 KVT[IS_A ? (32 * 20 * 40 + 64) : 8];
  const int tid = threadIdx.x, lane = tid & 63, w = tid >> 6;
  const int mt = blockIdx.x, nt = blockIdx.y;
  const int lr = lane & 15, lg = lane >> 4;
  const int row0 = mt * 32;
  if (IS_A) {
    for (int i = tid; i < 32 * 20 * 40; i += 256) {
      int h = i / 800, rem = i % 800, d = rem / 40, f = rem % 40;
      float v = (d < 19 && f < 36) ? kvacc[(h * 19 + d) * 36 + f] : 0.f;
      KVT[i] = f2bf(v);
    }
    __syncthreads();
    for (int hh = 0; hh < 8; ++hh) {
      const int h = w * 8 + hh;
      bf16x8 bb[2][2];
#pragma unroll
      for (int j = 0; j < 2; ++j) {
        int d = j * 16 + lr;
        int drow = d < 19 ? d : 19;
        bb[j][0] = *(const bf16x8*)(KVT + (h * 20 + drow) * 40 + lg * 8);
        bb[j][1] = *(const bf16x8*)(KVT + (h * 20 + (lg == 0 ? drow : 19)) * 40 + 32);
      }
#pragma unroll
      for (int i = 0; i < 2; ++i) {
        const u16* ap = phiQ + (size_t)(row0 + i * 16 + lr) * PQ_STRIDE + h * 40 + lg * 8;
        bf16x8 a0 = *(const bf16x8*)(ap);
        bf16x8 a1 = *(const bf16x8*)(ap + 32 - lg * 8);
        f32x4 c0 = {0.f, 0.f, 0.f, 0.f}, c1 = {0.f, 0.f, 0.f, 0.f};
        c0 = MFMA16(a0, bb[0][0], c0);
        c0 = MFMA16(a1, bb[0][1], c0);
        c1 = MFMA16(a0, bb[1][0], c1);
        c1 = MFMA16(a1, bb[1][1], c1);
#pragma unroll
        for (int r = 0; r < 4; ++r) {
          float den = __shfl(c1[r], (lane & 48) + 2);
          den = bf2f(f2bf(bf2f(f2bf(den)) + 1e-6f));
          float inv = 1.f / den;
          int rr = i * 16 + lg * 4 + r;
          O[rr * OSTR + h * DH + lr] = f2bf(bf2f(f2bf(c0[r])) * inv);
          if (lr < 2) O[rr * OSTR + h * DH + 16 + lr] = f2bf(bf2f(f2bf(c1[r])) * inv);
        }
      }
    }
  } else {
    for (int i = tid; i < 32 * 72; i += 256) {
      int r = i / 72, c = i % 72;
      *(bf16x8*)(O + r * OSTR + c * 8) = *(const bf16x8*)(oB + (size_t)(row0 + r) * DM + c * 8);
    }
  }
  __syncthreads();
  f32x4 acc[2][3];
#pragma unroll
  for (int i = 0; i < 2; ++i)
#pragma unroll
    for (int j = 0; j < 3; ++j) acc[i][j] = (f32x4){0.f, 0.f, 0.f, 0.f};
  const int col0 = nt * 192 + w * 48;
  const u16* wp = Wot + (size_t)(col0 + lr) * DM + lg * 8;
#pragma unroll 2
  for (int s = 0; s < 18; ++s) {
    bf16x8 a0 = *(const bf16x8*)(O + lr * OSTR + s * 32 + lg * 8);
    bf16x8 a1 = *(const bf16x8*)(O + (16 + lr) * OSTR + s * 32 + lg * 8);
#pragma unroll
    for (int j = 0; j < 3; ++j) {
      bf16x8 b = *(const bf16x8*)(wp + (size_t)j * 16 * DM + s * 32);
      acc[0][j] = MFMA16(a0, b, acc[0][j]);
      acc[1][j] = MFMA16(a1, b, acc[1][j]);
    }
  }
#pragma unroll
  for (int i = 0; i < 2; ++i)
#pragma unroll
    for (int j = 0; j < 3; ++j)
#pragma unroll
      for (int r = 0; r < 4; ++r) {
        size_t idx = (size_t)(row0 + i * 16 + lg * 4 + r) * DM + col0 + j * 16 + lr;
        float y = bf2f(f2bf(acc[i][j][r]));
        xout[idx] = f2bf(bf2f(xin[idx]) + y);
      }
}

#define KSTR 40
#define VSTR 136
__global__ __launch_bounds__(256) void attn_kernel(
    const u16* __restrict__ qB, const u16* __restrict__ kB, const u16* __restrict__ vT,
    u16* __restrict__ oB) {
  __shared__ __attribute__((aligned(16))) u16 Ks[2][128 * KSTR];
  __shared__ __attribute__((aligned(16))) u16 Vs[2][32 * VSTR];
  const int tid = threadIdx.x, lane = tid & 63, w = tid >> 6;
  const int lr = lane & 15, lg = lane >> 4;
  const int qt = blockIdx.x, h = blockIdx.y;
  const int q0 = qt * 128 + w * 32;
  const u16* Kg = kB + (size_t)h * S_LEN * 32;
  const u16* Vg = vT + (size_t)h * DH * S_LEN;

  for (int i = tid; i < 2 * 14 * 128; i += 256) {
    int b = i / (14 * 128), rem = i % (14 * 128);
    int d = 18 + rem / 128;
    Vs[b][d * VSTR + rem % 128] = (d == 18) ? (u16)0x3F80 : (u16)0;
  }
  auto load_tile = [&](int kt, int buf) {
#pragma unroll
    for (int i = 0; i < 2; ++i) {
      int c = tid + i * 256;
      *(bf16x8*)(&Ks[buf][(c >> 2) * KSTR + (c & 3) * 8]) =
          *(const bf16x8*)(Kg + (size_t)(kt * 128 + (c >> 2)) * 32 + (c & 3) * 8);
    }
#pragma unroll
    for (int i = 0; i < 2; ++i) {
      int c = tid + i * 256;
      if (c < 288)
        *(bf16x8*)(&Vs[buf][(c >> 4) * VSTR + (c & 15) * 8]) =
            *(const bf16x8*)(Vg + (size_t)(c >> 4) * S_LEN + kt * 128 + (c & 15) * 8);
    }
  };
  bf16x8 qf[2];
#pragma unroll
  for (int i = 0; i < 2; ++i)
    qf[i] = *(const bf16x8*)(qB + ((size_t)h * S_LEN + q0 + i * 16 + lr) * 32 + lg * 8);
  f32x4 o[2][2];
  float m[2];
#pragma unroll
  for (int i = 0; i < 2; ++i) {
    m[i] = -INFINITY;
    o[i][0] = (f32x4){0.f, 0.f, 0.f, 0.f};
    o[i][1] = (f32x4){0.f, 0.f, 0.f, 0.f};
  }
  load_tile(0, 0);
  __syncthreads();
  for (int kt = 0; kt < 16; ++kt) {
    const int buf = kt & 1;
    if (kt + 1 < 16) load_tile(kt + 1, buf ^ 1);
#pragma unroll
    for (int hf = 0; hf < 2; ++hf) {
      f32x4 s[2][4];
#pragma unroll
      for (int t = 0; t < 4; ++t) {
        bf16x8 kf = *(const bf16x8*)(&Ks[buf][(hf * 64 + t * 16 + lr) * KSTR + lg * 8]);
#pragma unroll
        for (int i = 0; i < 2; ++i) s[i][t] = MFMA16(kf, qf[i], ((f32x4){0.f, 0.f, 0.f, 0.f}));
      }
      bf16x8 vf[2][2];
#pragma unroll
      for (int u = 0; u < 2; ++u)
#pragma unroll
        for (int j = 0; j < 2; ++j) {
          const u16* vp = &Vs[buf][(j * 16 + lr) * VSTR + hf * 64 + u * 32 + lg * 4];
          ushort4 lo = *(const ushort4*)(vp);
          ushort4 hi = *(const ushort4*)(vp + 16);
          vf[u][j] = (bf16x8){(short)lo.x, (short)lo.y, (short)lo.z, (short)lo.w,
                              (short)hi.x, (short)hi.y, (short)hi.z, (short)hi.w};
        }
#pragma unroll
      for (int i = 0; i < 2; ++i) {
        float mx = s[i][0][0];
#pragma unroll
        for (int t = 0; t < 4; ++t)
#pragma unroll
          for (int r = 0; r < 4; ++r) mx = fmaxf(mx, s[i][t][r]);
        mx = fmaxf(mx, __shfl_xor(mx, 16));
        mx = fmaxf(mx, __shfl_xor(mx, 32));
        float mn = fmaxf(m[i], mx);
        float alpha = exp2f(m[i] - mn);
        m[i] = mn;
        o[i][0] *= alpha;
        o[i][1] *= alpha;
#pragma unroll
        for (int u = 0; u < 2; ++u) {
          u32 p[4];
          p[0] = cvtpk(exp2f(s[i][2 * u][0] - mn), exp2f(s[i][2 * u][1] - mn));
          p[1] = cvtpk(exp2f(s[i][2 * u][2] - mn), exp2f(s[i][2 * u][3] - mn));
          p[2] = cvtpk(exp2f(s[i][2 * u + 1][0] - mn), exp2f(s[i][2 * u + 1][1] - mn));
          p[3] = cvtpk(exp2f(s[i][2 * u + 1][2] - mn), exp2f(s[i][2 * u + 1][3] - mn));
          bf16x8 pf = __builtin_bit_cast(bf16x8, *(uint4*)p);
          o[i][0] = MFMA16(vf[u][0], pf, o[i][0]);
          o[i][1] = MFMA16(vf[u][1], pf, o[i][1]);
        }
      }
    }
    __syncthreads();
  }
#pragma unroll
  for (int i = 0; i < 2; ++i) {
    float l = __shfl(o[i][1][2], lr);
    float inv = 1.f / l;
    u16* dst = oB + (size_t)(q0 + i * 16 + lr) * DM + h * DH;
    u32 a = cvtpk(o[i][0][0] * inv, o[i][0][1] * inv);
    u32 b = cvtpk(o[i][0][2] * inv, o[i][0][3] * inv);
    *(uint2*)(dst + lg * 4) = make_uint2(a, b);
    if (lg == 0) *(u32*)(dst + 16) = cvtpk(o[i][1][0] * inv, o[i][1][1] * inv);
  }
}

extern "C" void kernel_launch(void* const* d_in, const int* in_sizes, int n_in,
                              void* d_out, int out_size, void* d_ws, size_t ws_size,
                              hipStream_t stream) {
  const u16* x0 = (const u16*)d_in[0];
  const u16* wqkvA = (const u16*)d_in[1];
  const u16* wphi = (const u16*)d_in[2];
  const u16* woA = (const u16*)d_in[3];
  const u16* wqkvB = (const u16*)d_in[5];
  const u16* woB = (const u16*)d_in[6];
  u16* out = (u16*)d_out;

  char* ws = (char*)d_ws;
  size_t off = 0;
  auto alloc = [&](size_t bytes) { void* p = ws + off; off += (bytes + 255) & ~(size_t)255; return p; };
  u16* WtA = (u16*)alloc((size_t)N3 * DM * 2);
  u16* WtB = (u16*)alloc((size_t)N3 * DM * 2);
  float* csA = (float*)alloc(N3 * 4);
  float* csB = (float*)alloc(N3 * 4);
  u16* woAt = (u16*)alloc((size_t)DM * DM * 2);
  u16* woBt = (u16*)alloc((size_t)DM * DM * 2);
  float* kvacc = (float*)alloc(2 * NH * 19 * 36 * 4);
  u16* phiQ = (u16*)alloc((size_t)S_LEN * PQ_STRIDE * 2 + 4096);
  u16* x1 = (u16*)alloc((size_t)S_LEN * DM * 2);
  u16* x2 = (u16*)alloc((size_t)S_LEN * DM * 2);
  u16* x3 = (u16*)alloc((size_t)S_LEN * DM * 2);
  u16* qB = (u16*)alloc((size_t)NH * S_LEN * 32 * 2);
  u16* kB = (u16*)alloc((size_t)NH * S_LEN * 32 * 2);
  u16* vT = (u16*)alloc((size_t)NH * DH * S_LEN * 2);
  u16* oB = (u16*)alloc((size_t)S_LEN * DM * 2);

  prep_kernel<<<355, 256, 0, stream>>>(wqkvA, wphi, woA, wqkvB, woB, WtA, csA, WtB, csB, woAt, woBt, kvacc);

  const u16* xin[4] = {x0, x1, x2, x3};
  u16* xo[4] = {x1, x2, x3, out};
  for (int layer = 0; layer < 2; ++layer) {
    float* kv = kvacc + layer * NH * 19 * 36;
    qkv_kernel<true><<<dim3(16, 12), 256, 0, stream>>>(xin[2 * layer], WtA, csA, phiQ, kv, nullptr, nullptr, nullptr);
    out_kernel<true><<<dim3(64, 3), 256, 0, stream>>>(phiQ, kv, nullptr, woAt, xin[2 * layer], xo[2 * layer]);
    qkv_kernel<false><<<dim3(16, 12), 256, 0, stream>>>(xin[2 * layer + 1], WtB, csB, nullptr, nullptr, qB, kB, vT);
    attn_kernel<<<dim3(16, 32), 256, 0, stream>>>(qB, kB, vT, oB);
    out_kernel<false><<<dim3(64, 3), 256, 0, stream>>>(nullptr, nullptr, oB, woBt, xin[2 * layer + 1], xo[2 * layer + 1]);
  }
}
```

```cpp
#include <hip/hip_runtime.h>
#include <hip/hip_bf16.h>
#include <stdint.h>

typedef __attribute__((ext_vector_type(8))) short bf16x8;
typedef __attribute__((ext_vector_type(4))) float f32x4;
typedef unsigned short u16;
typedef unsigned int u32;

#define S_LEN 2048
#define DM 576
#define NH 32
#define DH 18
#define N3 1728
#define PQ_STRIDE 1280

static __device__ __forceinline__ float bf2f(u16 v) { return __uint_as_float(((u32)v) << 16); }
static __device__ __forceinline__ u16 f2bf(float f) {
  u32 u = __float_as_uint(f);
  u += 0x7FFF + ((u >> 16) & 1);
  return (u16)(u >> 16);
}
static __device__ __forceinline__ u32 cvtpk(float lo, float hi) {
  typedef __attribute__((ext_vector_type(2))) float f32x2_;
  typedef __attribute__((ext_vector_type(2))) __bf16 bf16x2_;
  f32x2_ f = {lo, hi};
  return __builtin_bit_cast(u32, __builtin_convertvector(f, bf16x2_));
}
#define MFMA16(a, b, c) __builtin_amdgcn_mfma_f32_16x16x32_bf16(a, b, c, 0, 0, 0)

#define KV_H (20 * 40)
__global__ __launch_bounds__(256) void prep_kernel(
    const u16* __restrict__ wqkvA, const u16* __restrict__ wphi, const u16* __restrict__ woA,
    const u16* __restrict__ wqkvB, const u16* __restrict__ woB,
    u16* __restrict__ WtA, float* __restrict__ csA, u16* __restrict__ WtB, float* __restrict__ csB,
    u16* __restrict__ woAt, u16* __restrict__ woBt, float* __restrict__ kvacc) {
  __shared__ __attribute__((aligned(16))) float wpT[DH][20];
  __shared__ float cs[4][DH];
  __shared__ u16 tile[64][66];
  const int b = blockIdx.x, tid = threadIdx.x;
  if (b < 192) {
    const bool isA = b < 96;
    const int bb = isA ? b : b - 96;
    const int t = bb / NH, h = bb % NH;
    const u16* W = isA ? wqkvA : wqkvB;
    u32 raw[3][9];
#pragma unroll
    for (int i = 0; i < 3; ++i) {
      int k = tid + i * 256;
      if (k < DM) {
        const u32* src = (const u32*)(W + (size_t)k * N3 + t * DM + h * DH);
#pragma unroll
        for (int d = 0; d < 9; ++d) raw[i][d] = src[d];
      }
    }
    for (int i = tid; i < DH * DH; i += 256) {
      int d = i / DH, f = i - d * DH;
      wpT[f][d] = bf2f(wphi[h * DH * DH + i]);
    }
    __syncthreads();
    int nbase;
    if (isA) {
      if (t == 0) nbase = h * DH;
      else nbase = DM + (h / 4) * 144 + (t == 2 ? 72 : 0) + (h % 4) * DH;
    } else {
      nbase = t * DM + h * DH;
    }
    u16* Wt = isA ? WtA : WtB;
    const bool fold = isA && t < 2;
    float part[DH];
    float in[3][DH];
#pragma unroll
    for (int i = 0; i < 3; ++i)
#pragma unroll
      for (int d = 0; d < 9; ++d) {
        u32 rv = (tid + i * 256 < DM) ? raw[i][d] : 0u;
        in[i][2 * d] = __uint_as_float(rv << 16);
        in[i][2 * d + 1] = __uint_as_float(rv & 0xffff0000u);
      }
#pragma unroll
    for (int f = 0; f < DH; ++f) {
      float v[3];
      if (fold) {
        float4 w0 = *(const float4*)&wpT[f][0], w1 = *(const float4*)&wpT[f][4], w2 = *(const float4*)&wpT[f][8];
        float4 w3 = *(const float4*)&wpT[f][12], w4 = *(const float4*)&wpT[f][16];
#pragma unroll
        for (int i = 0; i < 3; ++i) {
          float a = in[i][0] * w0.x + in[i][1] * w0.y + in[i][2] * w0.z + in[i][3] * w0.w;
          a += in[i][4] * w1.x + in[i][5] * w1.y + in[i][6] * w1.z + in[i][7] * w1.w;
          a += in[i][8] * w2.x + in[i][9] * w2.y + in[i][10] * w2.z + in[i][11] * w2.w;
          a += in[i][12] * w3.x + in[i][13] * w3.y + in[i][14] * w3.z + in[i][15] * w3.w;
          a += in[i][16] * w4.x + in[i][17] * w4.y;
          v[i] = a;
        }
      } else {
#pragma unroll
        for (int i = 0; i < 3; ++i) v[i] = in[i][f];
      }
      part[f] = 0.f;
#pragma unroll
      for (int i = 0; i < 3; ++i) {
        int k = tid + i * 256;
        u16 r = f2bf(v[i]);
        if (k < DM) Wt[(size_t)(nbase + f) * DM + k] = r;
        part[f] += bf2f(r);
      }
    }
#pragma unroll
    for (int f = 0; f < DH; ++f) {
      float v = part[f];
#pragma unroll
      for (int o = 32; o > 0; o >>= 1) v += __shfl_xor(v, o);
      if ((tid & 63) == 0) cs[tid >> 6][f] = v;
    }
    __syncthreads();
    if (tid < DH) (isA ? csA : csB)[nbase + tid] = cs[0][tid] + cs[1][tid] + cs[2][tid] + cs[3][tid];
  } else if (b < 354) {
    const bool isA = b < 273;
    const int bb = isA ? b - 192 : b - 273;
    const int tr = bb / 9, tc = bb % 9;
    const u16* W = isA ? woA : woB;
    u16* Wt = isA ? woAt : woBt;
    for (int i = tid; i < 4096; i += 256) {
      int r = i / 64, c = i % 64;
      tile[r][c] = W[(size_t)(tr * 64 + r) * DM + tc * 64 + c];
    }
    __syncthreads();
    for (int i = tid; i < 4096; i += 256) {
      int r = i / 64, c = i % 64;
      Wt[(size_t)(tc * 64 + r) * DM + tr * 64 + c] = tile[c][r];
    }
  } else {
    float4* z = (float4*)kvacc;
    for (int i = tid + (b - 354) * 256; i < 2 * NH * KV_H / 4; i += 256 * 8) z[i] = make_float4(0.f, 0.f, 0.f, 0.f);
  }
}

#define BSTR 208
#define TSTR 152

static __device__ __forceinline__ void feat_map(const u16* src, u32* o  ) {
  float v[DH];
#pragma unroll
  for (int d = 0; d < 9; ++d) {
    u32 t = *(const u32*)(src + 2 * d);
    v[2 * d] = __uint_as_float(t << 16);
    v[2 * d + 1] = __uint_as_float(t & 0xffff0000u);
  }
  float mx = v[0], mn = v[0];
#pragma unroll
  for (int d = 1; d < DH; ++d) { mx = fmaxf(mx, v[d]); mn = fminf(mn, v[d]); }
  float ep[DH], en[DH], sp = 0.f, sn = 0.f;
#pragma unroll
  for (int d = 0; d < DH; ++d) {
    ep[d] = __expf(v[d] - mx); sp += ep[d];
    en[d] = __expf(mn - v[d]); sn += en[d];
  }
  float ip = 1.f / sp, in_ = 1.f / sn;
#pragma unroll
  for (int d = 0; d < 9; ++d) o[d] = cvtpk(ep[2 * d] * ip, ep[2 * d + 1] * ip);
#pragma unroll
  for (int d = 0; d < 9; ++d) o[9 + d] = cvtpk(en[2 * d] * in_, en[2 * d + 1] * in_);
}

#define QBSTR 80
template <bool IS_A>
__global__ __launch_bounds__(256, 2) void qkv_kernel(
    const u16* __restrict__ x, const u16* __restrict__ Wt, const float* __restrict__ colsum,
    u16* __restrict__ phiQ, u16* __restrict__ kvT,
    u16* __restrict__ qB, u16* __restrict__ kB, u16* __restrict__ vT) {
  __shared__ __attribute__((aligned(16))) unsigned char smem[2 * 144 * QBSTR * 2];
  u16* Bs = (u16*)smem;
  const int tid = threadIdx.x, lane = tid & 63, w = tid >> 6;
  const int xcd = blockIdx.x & 7, idx = blockIdx.x >> 3;
  const int mt = (xcd & 3) * 8 + (idx & 7), nt = (xcd >> 2) * 6 + (idx >> 3);
  const int row0 = mt * 64 + w * 16;
  const int lr = lane & 15, lg = lane >> 4;

  f32x4 acc[9];
#pragma unroll
  for (int j = 0; j < 9; ++j) acc[j] = (f32x4){0.f, 0.f, 0.f, 0.f};
  float s1 = 0.f, s2 = 0.f;

  const u16* xa = x + (size_t)(row0 + lr) * DM + lg * 8;
  const u16* Wg = Wt + (size_t)(nt * 144 + (tid >> 3)) * DM + (tid & 7) * 8;
  const int bso = (tid >> 3) * QBSTR + (tid & 7) * 8;
  bf16x8 rb[2][5], ra[2][2], a0, a1;
  auto gload = [&](int st, int rs) {
#pragma unroll
    for (int i = 0; i < 4; ++i) rb[rs][i] = *(const bf16x8*)(Wg + (size_t)i * 32 * DM + st * 64);
    if (tid < 128) rb[rs][4] = *(const bf16x8*)(Wg + (size_t)4 * 32 * DM + st * 64);
    ra[rs][0] = *(const bf16x8*)(xa + st * 64);
    ra[rs][1] = *(const bf16x8*)(xa + st * 64 + 32);
  };
  auto sstore = [&](int buf, int rs) {
#pragma unroll
    for (int i = 0; i < 4; ++i) *(bf16x8*)(Bs + buf * 144 * QBSTR + bso + i * 32 * QBSTR) = rb[rs][i];
    if (tid < 128) *(bf16x8*)(Bs + buf * 144 * QBSTR + bso + 4 * 32 * QBSTR) = rb[rs][4];
  };
  f32x4 csv[9];
#pragma unroll
  for (int j = 0; j < 9; ++j) csv[j] = *(const f32x4*)(colsum + nt * 144 + j * 16 + lg * 4);
  gload(0, 0);
  gload(1, 1);
  sstore(0, 0);
  a0 = ra[0][0]; a1 = ra[0][1];
  gload(2, 0);
  __syncthreads();
#pragma unroll
  for (int st = 0; st < 9; ++st) {
    const int buf = st & 1;
    const u16* Bb = Bs + buf * 144 * QBSTR + lr * QBSTR + lg * 8;
#pragma unroll
    for (int j = 0; j < 9; ++j) {
      bf16x8 b0 = *(const bf16x8*)(Bb + j * 16 * QBSTR);
      bf16x8 b1 = *(const bf16x8*)(Bb + j * 16 * QBSTR + 32);
      acc[j] = MFMA16(b0, a0, acc[j]);
      acc[j] = MFMA16(b1, a1, acc[j]);
    }
#pragma unroll
    for (int e = 0; e < 8; ++e) {
      float f = bf2f((u16)a0[e]), g = bf2f((u16)a1[e]);
      s1 += f + g;
      s2 += f * f + g * g;
    }
    if (st + 1 < 9) {
      sstore(buf ^ 1, buf ^ 1);
      a0 = ra[buf ^ 1][0]; a1 = ra[buf ^ 1][1];
      if (st + 3 < 9) gload(st + 3, buf ^ 1);
    }
    __syncthreads();
  }
  u16* T = (u16*)smem;
  {
    s1 += __shfl_xor(s1, 16); s1 += __shfl_xor(s1, 32);
    s2 += __shfl_xor(s2, 16); s2 += __shfl_xor(s2, 32);
    float m = s1 * (1.f / DM);
    float v = fmaxf(s2 * (1.f / DM) - m * m, 0.f);
    float rs = rsqrtf(v + 1e-5f);
#pragma unroll
    for (int j = 0; j < 9; ++j) {
      f32x4 cs = csv[j];
      float v0 = (acc[j][0] - m * cs[0]) * rs, v1 = (acc[j][1] - m * cs[1]) * rs;
      float v2 = (acc[j][2] - m * cs[2]) * rs, v3 = (acc[j][3] - m * cs[3]) * rs;
      *(uint2*)(T + (w * 16 + lr) * TSTR + j * 16 + lg * 4) = make_uint2(cvtpk(v0, v1), cvtpk(v2, v3));
    }
  }
  __syncthreads();
  if (IS_A) {
    if (nt < 4) {
#pragma unroll
      for (int it = 0; it < 2; ++it) {
        int task = tid + it * 256;
        int r = task >> 3, hh = task & 7;
        u32 o[18];
        feat_map(T + r * TSTR + hh * DH, o);
        u32* dst = (u32*)(phiQ + (size_t)(mt * 64 + r) * PQ_STRIDE + (nt * 8 + hh) * 40);
#pragma unroll
        for (int q = 0; q < 4; ++q)
          *(uint4*)(dst + q * 4) = make_uint4(o[q * 4], o[q * 4 + 1], o[q * 4 + 2], o[q * 4 + 3]);
        *(uint4*)(dst + 16) = make_uint4(o[16], o[17], 0u, 0u);
      }
    } else {
      u16* PK = (u16*)(smem + 64 * TSTR * 2);
      const int r = tid >> 2, hh = tid & 3;
      const u16* src = T + r * TSTR + hh * DH;
      u32 o[18], vv[9];
      feat_map(src, o);
#pragma unroll
      for (int d = 0; d < 9; ++d) vv[d] = *(const u32*)(src + 72 + 2 * d);
      __syncthreads();
      PK = (u16*)smem;
#pragma unroll
      for (int d = 0; d < 18; ++d) {
        PK[(hh * 54 + 2 * d) * 72 + r] = (u16)(o[d] & 0xffff);
        PK[(hh * 54 + 2 * d + 1) * 72 + r] = (u16)(o[d] >> 16);
      }
#pragma unroll
      for (int d = 0; d < 9; ++d) {
        PK[(hh * 54 + 36 + 2 * d) * 72 + r] = (u16)(vv[d] & 0xffff);
        PK[(hh * 54 + 36 + 2 * d + 1) * 72 + r] = (u16)(vv[d] >> 16);
      }
      __syncthreads();
      for (int i = tid; i < 4 * 54 * 8; i += 256) {
        int row = i >> 3, c8 = i & 7;
        int h4 = row / 54, rr = row - h4 * 54;
        uint4 val = *(const uint4*)(PK + row * 72 + c8 * 8);
        *(uint4*)(kvT + ((size_t)((nt - 4) * 4 + h4) * 54 + rr) * S_LEN + mt * 64 + c8 * 8) = val;
      }
    }
  } else {
    const int t = nt / 4, hb = (nt % 4) * 8;
    if (t < 2) {
      const float sc = (t == 0) ? 0.23570226039551584f * 1.4426950408889634f : 1.f;
      u16* dstb = (t == 0) ? qB : kB;
#pragma unroll
      for (int it = 0; it < 2; ++it) {
        int task = tid + it * 256;
        int hh = task >> 6, r = task & 63;
        const u16* src = T + r * TSTR + hh * DH;
        u32 o[9];
#pragma unroll
        for (int d = 0; d < 9; ++d) {
          u32 tt = *(const u32*)(src + 2 * d);
          o[d] = (t == 0) ? cvtpk(__uint_as_float(tt << 16) * sc, __uint_as_float(tt & 0xffff0000u) * sc) : tt;
        }
        u32* dst = (u32*)(dstb + ((size_t)(hb + hh) * S_LEN + mt * 64 + r) * 32);
        *(uint4*)(dst) = make_uint4(o[0], o[1], o[2], o[3]);
        *(uint4*)(dst + 4) = make_uint4(o[4], o[5], o[6], o[7]);
        *(uint4*)(dst + 8) = make_uint4(o[8], 0u, 0u, 0u);
        *(uint4*)(dst + 12) = make_uint4(0u, 0u, 0u, 0u);
      }
    } else {
      for (int i = tid; i < 144 * 8; i += 256) {
        int col = i >> 3, c8 = i & 7;
        int base = (c8 >> 1) * 16 + (c8 & 1) * 4;
        u32 o[4];
#pragma unroll
        for (int q = 0; q < 4; ++q) {
          int k0 = base + 8 * (q >> 1) + 2 * (q & 1);
          o[q] = (u32)T[k0 * TSTR + col] | ((u32)T[(k0 + 1) * TSTR + col] << 16);
        }
        *(uint4*)(vT + ((size_t)(hb * DH + col)) * S_LEN + mt * 64 + c8 * 8) = make_uint4(o[0], o[1], o[2], o[3]);
      }
    }
  }
}

__global__ __launch_bounds__(512) void kv_kernel(const u16* __restrict__ kvT, u16* __restrict__ kvb) {
  __shared__ float red[8][48][33];
  const int tid = threadIdx.x, lane = tid & 63, w = tid >> 6;
  const int lr = lane & 15, lg = lane >> 4;
  const int h = blockIdx.x;
  const u16* base = kvT + (size_t)h * 54 * S_LEN + w * 256 + lg * 8;
  f32x4 c[3][2];
#pragma unroll
  for (int i = 0; i < 3; ++i)
#pragma unroll
    for (int j = 0; j < 2; ++j) c[i][j] = (f32x4){0.f, 0.f, 0.f, 0.f};
  const int f2 = min(32 + lr, 35);
  const bool v1ok = lr < 2;
  const short one = (short)0x3F80;
  bf16x8 cst = (lr == 2) ? (bf16x8){one, one, one, one, one, one, one, one} : (bf16x8){0, 0, 0, 0, 0, 0, 0, 0};
#pragma unroll
  for (int s = 0; s < 8; ++s) {
    bf16x8 a0 = *(const bf16x8*)(base + (size_t)lr * S_LEN + s * 32);
    bf16x8 a1 = *(const bf16x8*)(base + (size_t)(16 + lr) * S_LEN + s * 32);
    bf16x8 a2 = *(const bf16x8*)(base + (size_t)f2 * S_LEN + s * 32);
    bf16x8 b0 = *(const bf16x8*)(base + (size_t)(36 + lr) * S_LEN + s * 32);
    bf16x8 b1 = *(const bf16x8*)(base + (size_t)(36 + 16 + (v1ok ? lr : 0)) * S_LEN + s * 32);
    if (!v1ok) b1 = cst;
    c[0][0] = MFMA16(a0, b0, c[0][0]); c[0][1] = MFMA16(a0, b1, c[0][1]);
    c[1][0] = MFMA16(a1, b0, c[1][0]); c[1][1] = MFMA16(a1, b1, c[1][1]);
    c[2][0] = MFMA16(a2, b0, c[2][0]); c[2][1] = MFMA16(a2, b1, c[2][1]);
  }
#pragma unroll
  for (int i = 0; i < 3; ++i)
#pragma unroll
    for (int j = 0; j < 2; ++j)
#pragma unroll
      for (int r = 0; r < 4; ++r) red[w][i * 16 + lg * 4 + r][j * 16 + lr] = c[i][j][r];
  __syncthreads();
  for (int i = tid; i < 800; i += 512) {
    int d = i / 40, f = i - d * 40;
    float v = 0.f;
    if (d < 19 && f < 36) {
#pragma unroll
      for (int k = 0; k < 8; ++k) v += red[k][f][d];
    }
    kvb[h * 800 + i] = f2bf(v);
  }
}

__global__ __launch_bounds__(256) void o_kernel(const u16* __restrict__ phiQ, const u16* __restrict__ kvb,
                                                u16* __restrict__ oB) {
  __shared__ __attribute__((aligned(16))) u16 KVT[8 * 800 + 64];
  const int tid = threadIdx.x, lane = tid & 63, w = tid >> 6;
  const int lr = lane & 15, lg = lane >> 4;
  const int mt = blockIdx.x >> 2, hg = blockIdx.x & 3;
  const int row0 = mt * 32;
  bf16x8 pa[2][2][2];
#pragma unroll
  for (int hh = 0; hh < 2; ++hh)
#pragma unroll
    for (int i = 0; i < 2; ++i) {
      const u16* ap = phiQ + (size_t)(row0 + i * 16 + lr) * PQ_STRIDE + (hg * 8 + w * 2 + hh) * 40;
      pa[hh][i][0] = *(const bf16x8*)(ap + lg * 8);
      pa[hh][i][1] = *(const bf16x8*)(ap + 32);
    }
  {
    uint4 t0 = ((const uint4*)(kvb + hg * 6400))[tid], t1 = ((const uint4*)(kvb + hg * 6400))[tid + 256];
    uint4 t2 = ((const uint4*)(kvb + hg * 6400))[tid + 512], t3 = make_uint4(0u, 0u, 0u, 0u);
    if (tid < 32) t3 = ((const uint4*)(kvb + hg * 6400))[tid + 768];
    ((uint4*)KVT)[tid] = t0; ((uint4*)KVT)[tid + 256] = t1; ((uint4*)KVT)[tid + 512] = t2;
    if (tid < 32) ((uint4*)KVT)[tid + 768] = t3;
  }
  __syncthreads();
#pragma unroll
  for (int hh = 0; hh < 2; ++hh) {
    const int hl = w * 2 + hh, h = hg * 8 + hl;
    bf16x8 bb[2][2];
#pragma unroll
    for (int j = 0; j < 2; ++j) {
      int d = j * 16 + lr;
      int drow = d < 19 ? d : 19;
      bb[j][0] = *(const bf16x8*)(KVT + (hl * 20 + drow) * 40 + lg * 8);
      bb[j][1] = *(const bf16x8*)(KVT + (hl * 20 + (lg == 0 ? drow : 19)) * 40 + 32);
    }
#pragma unroll
    for (int i = 0; i < 2; ++i) {
      f32x4 c0 = {0.f, 0.f, 0.f, 0.f}, c1 = {0.f, 0.f, 0.f, 0.f};
      c0 = MFMA16(pa[hh][i][0], bb[0][0], c0);
      c0 = MFMA16(pa[hh][i][1], bb[0][1], c0);
      c1 = MFMA16(pa[hh][i][0], bb[1][0], c1);
      c1 = MFMA16(pa[hh][i][1], bb[1][1], c1);
#pragma unroll
      for (int r = 0; r < 4; ++r) {
        float den = __shfl(c1[r], (lane & 48) + 2);
        den = bf2f(f2bf(bf2f(f2bf(den)) + 1e-6f));
        float inv = __builtin_amdgcn_rcpf(den);
        u16* dst = oB + (size_t)(row0 + i * 16 + lg * 4 + r) * DM + h * DH;
        dst[lr] = f2bf(bf2f(f2bf(c0[r])) * inv);
        if (lr < 2) dst[16 + lr] = f2bf(bf2f(f2bf(c1[r])) * inv);
      }
    }
  }
}

#define OSTR 584
template <bool IS_A>
__global__ __launch_bounds__(256, 1) void out_kernel(
    const u16* __restrict__ phiQ, const u16* __restrict__ kvb, const u16* __restrict__ oB,
    const u16* __restrict__ Wot, const u16* __restrict__ xin, u16* __restrict__ xout) {
  __shared__ __attribute__((aligned(16))) u16 O[32 * OSTR];
  __shared__ __attribute__((aligned(16))) u16 KVT[IS_A ? (32 * 20 * 40 + 64) : 8];
  const int tid = threadIdx.x, lane = tid & 63, w = tid >> 6;
  const int xcd = blockIdx.x & 7, idx = blockIdx.x >> 3;
  const int mt = xcd * 8 + (idx & 7), nt = idx >> 3;
  const int lr = lane & 15, lg = lane >> 4;
  const int row0 = mt * 32;
  const int col0 = nt * 192 + w * 48;
  bf16x8 wb[3][18];
  {
    const u16* wp = Wot + (size_t)(col0 + lr) * DM + lg * 8;
#pragma unroll
    for (int j = 0; j < 3; ++j)
#pragma unroll
      for (int s = 0; s < 18; ++s) wb[j][s] = *(const bf16x8*)(wp + (size_t)j * 16 * DM + s * 32);
  }
  if (IS_A) {
    bf16x8 pa[8][2][2];
#pragma unroll
    for (int hh = 0; hh < 8; ++hh)
#pragma unroll
      for (int i = 0; i < 2; ++i) {
        const u16* ap = phiQ + (size_t)(row0 + i * 16 + lr) * PQ_STRIDE + (w * 8 + hh) * 40;
        pa[hh][i][0] = *(const bf16x8*)(ap + lg * 8);
        pa[hh][i][1] = *(const bf16x8*)(ap + 32);
      }
    {
      uint4 t[12], t12 = make_uint4(0u, 0u, 0u, 0u);
#pragma unroll
      for (int i = 0; i < 12; ++i) t[i] = ((const uint4*)kvb)[tid + i * 256];
      if (tid < 128) t12 = ((const uint4*)kvb)[tid + 3072];
#pragma unroll
      for (int i = 0; i < 12; ++i) ((uint4*)KVT)[tid + i * 256] = t[i];
      if (tid < 128) ((uint4*)KVT)[tid + 3072] = t12;
    }
    __syncthreads();
#pragma unroll
    for (int hh = 0; hh < 8; ++hh) {
      const int h = w * 8 + hh;
      bf16x8 bb[2][2];
#pragma unroll
      for (int j = 0; j < 2; ++j) {
        int d = j * 16 + lr;
        int drow = d < 19 ? d : 19;
        bb[j][0] = *(const bf16x8*)(KVT + (h * 20 + drow) * 40 + lg * 8);
        bb[j][1] = *(const bf16x8*)(KVT + (h * 20 + (lg == 0 ? drow : 19)) * 40 + 32);
      }
#pragma unroll
      for (int i = 0; i < 2; ++i) {
        f32x4 c0 = {0.f, 0.f, 0.f, 0.f}, c1 = {0.f, 0.f, 0.f, 0.f};
        c0 = MFMA16(pa[hh][i][0], bb[0][0], c0);
        c0 = MFMA16(pa[hh][i][1], bb[0][1], c0);
        c1 = MFMA16(pa[hh][i][0], bb[1][0], c1);
        c1 = MFMA16(pa[hh][i][1], bb[1][1], c1);
#pragma unroll
        for (int r = 0; r < 4; ++r) {
          float den = __shfl(c1[r], (lane & 48) + 2);
          den = bf2f(f2bf(bf2f(f2bf(den)) + 1e-6f));
          float inv = __builtin_amdgcn_rcpf(den);
          int rr = i * 16 + lg * 4 + r;
          O[rr * OSTR + h * DH + lr] = f2bf(bf2f(f2bf(c0[r])) * inv);
          if (lr < 2) O[rr * OSTR + h * DH + 16 + lr] = f2bf(bf2f(f2bf(c1[r])) * inv);
        }
      }
    }
  } else {
    bf16x8 t[9];
#pragma unroll
    for (int i = 0; i < 9; ++i) {
      int c = tid + i * 256, r = c / 72, cc = c - r * 72;
      t[i] = *(const bf16x8*)(oB + (size_t)(row0 + r) * DM + cc * 8);
    }
#pragma unroll
    for (int i = 0; i < 9; ++i) {
      int c = tid + i * 256, r = c / 72, cc = c - r * 72;
      *(bf16x8*)(O + r * OSTR + cc * 8) = t[i];
    }
  }
  u16 xr[2][3][4];
#pragma unroll
  for (int i = 0; i < 2; ++i)
#pragma unroll
    for (int j = 0; j < 3; ++j)
#pragma unroll
      for (int r = 0; r < 4; ++r)
        xr[i][j][r] = xin[(size_t)(row0 + i * 16 + lg * 4 + r) * DM + col0 + j * 16 + lr];
  __syncthreads();
  f32x4 acc[2][3];
#pragma unroll
  for (int i = 0; i < 2; ++i)
#pragma unroll
    for (int j = 0; j < 3; ++j) acc[i][j] = (f32x4){0.f, 0.f, 0.f, 0.f};
#pragma unroll
  for (int s = 0; s < 18; ++s) {
    bf16x8 a0 = *(const bf16x8*)(O + lr * OSTR + s * 32 + lg * 8);
    bf16x8 a1 = *(const bf16x8*)(O + (16 + lr) * OSTR + s * 32 + lg * 8);
#pragma unroll
    for (int j = 0; j < 3; ++j) {
      acc[0][j] = MFMA16(a0, wb[j][s], acc[0][j]);
      acc[1][j] = MFMA16(a1, wb[j][s], acc[1][j]);
    }
  }
#pragma unroll
  for (int i = 0; i < 2; ++i)
#pragma unroll
    for (int j = 0; j < 3; ++j)
#pragma unroll
      for (int r = 0; r < 4; ++r) {
        size_t idx2 = (size_t)(row0 + i * 16 + lg * 4 + r) * DM + col0 + j * 16 + lr;
        float y = bf2f(f2bf(acc[i][j][r]));
        xout[idx2] = f2bf(bf2f(xr[i][j][r]) + y);
      }
}

typedef __attribute__((ext_vector_type(16))) float f32x16;
#define MFMA32(a, b, c) __builtin_amdgcn_mfma_f32_32x32x16_bf16(a, b, c, 0, 0, 0)
#define KSTR 40
#define VSTR 72
#define EXP2(x) __builtin_amdgcn_exp2f(x)
#define ATHR 8.0f
#define NKS 2
#define NST (32 / NKS)
__global__ __launch_bounds__(256 * NKS, 4) void attn_kernel(
    const u16* __restrict__ qB, const u16* __restrict__ kB, const u16* __restrict__ vT,
    u16* __restrict__ oB) {
  __shared__ __attribute__((aligned(16))) u16 Ks[3][NKS][64 * KSTR];
  __shared__ __attribute__((aligned(16))) u16 Vs[3][NKS][32 * VSTR];
  const int tid = threadIdx.x, lane = tid & 63, w = tid >> 6;
  const int l31 = lane & 31, hh = lane >> 5;
  const int qg = w & 3, ks = w >> 2;
  const int qt = blockIdx.x, h = blockIdx.y;
  const int q0 = qt * 128 + qg * 32;
  const int lkh = tid >> 8, lkr = (tid >> 2) & 63, lkc = tid & 3;
  const u16* Kg = kB + (size_t)h * S_LEN * 32 + (size_t)(lkh * (S_LEN / NKS) + lkr) * 32 + lkc * 8;
  const int kso = lkh * 64 * KSTR + lkr * KSTR + lkc * 8;
  const int lvh = tid / 144, lvr = (tid % 144) >> 3, lvc = tid & 7;
  const u16* Vg = vT + (size_t)h * DH * S_LEN + (size_t)lvr * S_LEN + lvh * (S_LEN / NKS) + lvc * 8;
  const int vso = lvh * 32 * VSTR + lvr * VSTR + lvc * 8;
  const bool vl = tid < 144 * NKS;

  bf16x8 rk, rv, rk1, rv1, rk2, rv2;
  rk1 = *(const bf16x8*)(Kg);
  rk2 = *(const bf16x8*)(Kg + 64 * 32);
  rk = *(const bf16x8*)(Kg + 2 * 64 * 32);
  if (vl) {
    rv1 = *(const bf16x8*)(Vg);
    rv2 = *(const bf16x8*)(Vg + 64);
    rv = *(const bf16x8*)(Vg + 128);
  }
  bf16x8 qf[2];
#pragma unroll
  for (int s = 0; s < 2; ++s)
    qf[s] = *(const bf16x8*)(qB + ((size_t)h * S_LEN + q0 + l31) * 32 + s * 16 + hh * 8);
  for (int i = tid; i < 3 * NKS * 14 * 32; i += 256 * NKS) {
    int b = i / (14 * 32), rem = i - b * (14 * 32);
    int d = 18 + (rem >> 5);
    *(u32*)(&Vs[0][0][0] + b * 32 * VSTR + d * VSTR + (rem & 31) * 2) = (d == 18) ? 0x3F803F80u : 0u;
  }
  *(bf16x8*)(&Ks[0][0][kso]) = rk1;
  *(bf16x8*)(&Ks[1][0][kso]) = rk2;
  if (vl) {
    *(bf16x8*)(&Vs[0][0][vso]) = rv1;
    *(bf16x8*)(&Vs[1][0][vso]) = rv2;
  }
  auto gload = [&](int st) {
    rk = *(const bf16x8*)(Kg + (size_t)st * 64 * 32);
    if (vl) rv = *(const bf16x8*)(Vg + st * 64);
  };
  auto sstore = [&](int buf) {
    *(bf16x8*)(&Ks[buf][0][kso]) = rk;
    if (vl) *(bf16x8*)(&Vs[buf][0][vso]) = rv;
  };
  f32x16 o, negm;
#pragma unroll
  for (int r = 0; r < 16; ++r) { o[r] = 0.f; negm[r] = 0.f; }
  float mref = 0.f;
  __syncthreads();
  auto qk = [&](int buf, int half, f32x16& a0) {
    const u16* Kb = &Ks[buf][ks][half * 32 * KSTR];
    bf16x8 k00 = *(const bf16x8*)(Kb + l31 * KSTR + hh * 8);
    bf16x8 k01 = *(const bf16x8*)(Kb + l31 * KSTR + 16 + hh * 8);
    a0 = MFMA32(k00, qf[0], negm);
    a0 = MFMA32(k01, qf[1], a0);
  };
  auto smpv = [&](f32x16& sc, f32x16& so, const u16* Vb, bool first) {
    float m0 = fmaxf(fmaxf(sc[0], sc[1]), sc[2]);
    float m1 = fmaxf(fmaxf(sc[3], sc[4]), sc[5]);
    float m2 = fmaxf(fmaxf(sc[6], sc[7]), sc[8]);
    float m3 = fmaxf(fmaxf(sc[9], sc[10]), sc[11]);
    m0 = fmaxf(fmaxf(m0, sc[12]), sc[13]);
    m1 = fmaxf(fmaxf(m1, sc[14]), sc[15]);
    float mx = fmaxf(fmaxf(m0, m1), fmaxf(m2, m3));
    {
      auto rr = __builtin_amdgcn_permlane32_swap(__float_as_uint(mx), __float_as_uint(mx), false, false);
      mx = fmaxf(__uint_as_float(rr[0]), __uint_as_float(rr[1]));
    }
    if (first || __builtin_amdgcn_ballot_w64(mx > ATHR) != 0) {
      float delta = first ? mx : fmaxf(mx, 0.f);
      float alpha = first ? 1.f : EXP2(-delta);
      mref += delta;
#pragma unroll
      for (int r = 0; r < 16; ++r) {
        negm[r] = -mref;
        sc[r] -= delta;
        so[r] -= delta;
      }
#pragma unroll
      for (int r = 0; r < 12; ++r) o[r] *= alpha;
    }
    u32 p[8];
#pragma unroll
    for (int r = 0; r < 8; ++r) p[r] = cvtpk(EXP2(sc[2 * r]), EXP2(sc[2 * r + 1]));
#pragma unroll
    for (int u = 0; u < 2; ++u) {
      bf16x8 vf = *(const bf16x8*)(Vb + l31 * VSTR + u * 16 + hh * 8);
      bf16x8 pf = __builtin_bit_cast(bf16x8, *(uint4*)(p + 4 * u));
      o = MFMA32(vf, pf, o);
    }
  };
  f32x16 sa, sb;
  qk(0, 0, sa);
  int bc = 0;
  for (int st = 0; st < NST; ++st) {
    const int bn = (bc == 2) ? 0 : bc + 1;
    const int bs = (bn == 2) ? 0 : bn + 1;
    const u16* Vb = &Vs[bc][ks][0];
    qk(bc, 1, sb);
    smpv(sa, sb, Vb, st == 0);
    if (st + 1 < NST) qk(bn, 0, sa);
    smpv(sb, sa, Vb + 32, false);
    if (st + 2 < NST) sstore(bs);
    if (st + 3 < NST) gload(st + 3);
    __syncthreads();
    bc = bn;
  }
  float* cb = (float*)&Ks[0][0][0];
  if (ks != 0) {
    float* c = cb + ((ks - 1) * 4 + qg) * 13 * 64;
#pragma unroll
    for (int r = 0; r < 12; ++r) c[r * 64 + lane] = o[r];
    c[12 * 64 + lane] = mref;
  }
  __syncthreads();
  if (ks == 0) {
    float mn = mref;
    float mo[NKS - 1];
#pragma unroll
    for (int k = 0; k < NKS - 1; ++k) { mo[k] = cb[((k * 4 + qg) * 13 + 12) * 64 + lane]; mn = fmaxf(mn, mo[k]); }
    float a0 = EXP2(mref - mn);
#pragma unroll
    for (int r = 0; r < 12; ++r) o[r] *= a0;
#pragma unroll
    for (int k = 0; k < NKS - 1; ++k) {
      float a1 = EXP2(mo[k] - mn);
#pragma unroll
      for (int r = 0; r < 12; ++r) o[r] += cb[((k * 4 + qg) * 13 + r) * 64 + lane] * a1;
    }
    float l = __shfl(o[10], l31);
    float inv = 1.f / l;
    u16* dst = oB + (size_t)(q0 + l31) * DM + h * DH;
    *(uint2*)(dst + 4 * hh) = make_uint2(cvtpk(o[0] * inv, o[1] * inv), cvtpk(o[2] * inv, o[3] * inv));
    *(uint2*)(dst + 8 + 4 * hh) = make_uint2(cvtpk(o[4] * inv, o[5] * inv), cvtpk(o[6] * inv, o[7] * inv));
    if (hh == 0) *(u32*)(dst + 16) = cvtpk(o[8] * inv, o[9] * inv);
  }
}

extern "C" void kernel_launch(void* const* d_in, const int* in_sizes, int n_in,
                              void* d_out, int out_size, void* d_ws, size_t ws_size,
                              hipStream_t stream) {
  const u16* x0 = (const u16*)d_in[0];
  const u16* wqkvA = (const u16*)d_in[1];
  const u16* wphi = (const u16*)d_in[2];
  const u16* woA = (const u16*)d_in[3];
  const u16* wqkvB = (const u16*)d_in[5];
  const u16* woB = (const u16*)d_in[6];
  u16* out = (u16*)d_out;

  char* ws = (char*)d_ws;
  size_t off = 0;
  auto alloc = [&](size_t bytes) { void* p = ws + off; off += (bytes + 255) & ~(size_t)255; return p; };
  u16* WtA = (u16*)alloc((size_t)N3 * DM * 2);
  u16* WtB = (u16*)alloc((size_t)N3 * DM * 2);
  float* csA = (float*)alloc(N3 * 4);
  float* csB = (float*)alloc(N3 * 4);
  u16* woAt = (u16*)alloc((size_t)DM * DM * 2);
  u16* woBt = (u16*)alloc((size_t)DM * DM * 2);
  float* kvacc = (float*)alloc(2 * NH * KV_H * 4);
  u16* kvT = (u16*)alloc((size_t)NH * 54 * S_LEN * 2);
  u16* kvb = (u16*)alloc(NH * 800 * 2);
  u16* phiQ = (u16*)alloc((size_t)S_LEN * PQ_STRIDE * 2 + 4096);
  u16* x1 = (u16*)alloc((size_t)S_LEN * DM * 2);
  u16* x2 = (u16*)alloc((size_t)S_LEN * DM * 2);
  u16* x3 = (u16*)alloc((size_t)S_LEN * DM * 2);
  u16* qB = (u16*)alloc((size_t)NH * S_LEN * 32 * 2);
  u16* kB = (u16*)alloc((size_t)NH * S_LEN * 32 * 2);
  u16* vT = (u16*)alloc((size_t)NH * DH * S_LEN * 2);
  u16* oB = (u16*)alloc((size_t)S_LEN * DM * 2);

  prep_kernel<<<362, 256, 0, stream>>>(wqkvA, wphi, woA, wqkvB, woB, WtA, csA, WtB, csB, woAt, woBt, kvacc);

  const u16* xin[4] = {x0, x1, x2, x3};
  u16* xo[4] = {x1, x2, x3, out};
  for (int layer = 0; layer < 2; ++layer) {
    qkv_kernel<true><<<384, 256, 0, stream>>>(xin[2 * layer], WtA, csA, phiQ, kvT, nullptr, nullptr, nullptr);
    kv_kernel<<<32, 512, 0, stream>>>(kvT, kvb);
    o_kernel<<<256, 256, 0, stream>>>(phiQ, kvb, oB);
    out_kernel<false><<<192, 256, 0, stream>>>(nullptr, nullptr, oB, woAt, xin[2 * layer], xo[2 * layer]);
    qkv_kernel<false><<<384, 256, 0, stream>>>(xin[2 * layer + 1], WtB, csB, nullptr, nullptr, qB, kB, vT);
    attn_kernel<<<dim3(16, 32), 256 * NKS, 0, stream>>>(qB, kB, vT, oB);
    out_kernel<false><<<192, 256, 0, stream>>>(nullptr, nullptr, oB, woBt, xin[2 * layer + 1], xo[2 * layer + 1]);
  }
}
```

```cpp
#include <hip/hip_runtime.h>
#include <hip/hip_bf16.h>
#include <stdint.h>

typedef __attribute__((ext_vector_type(8))) short bf16x8;
typedef __attribute__((ext_vector_type(4))) float f32x4;
typedef unsigned short u16;
typedef unsigned int u32;

#define S_LEN 2048
#define DM 576
#define NH 32
#define DH 18
#define N3 1728
#define PQ_STRIDE 1280

static __device__ __forceinline__ float bf2f(u16 v) { return __uint_as_float(((u32)v) << 16); }
static __device__ __forceinline__ u16 f2bf(float f) {
  u32 u = __float_as_uint(f);
  u += 0x7FFF + ((u >> 16) & 1);
  return (u16)(u >> 16);
}
static __device__ __forceinline__ u32 cvtpk(float lo, float hi) {
  typedef __attribute__((ext_vector_type(2))) float f32x2_;
  typedef __attribute__((ext_vector_type(2))) __bf16 bf16x2_;
  f32x2_ f = {lo, hi};
  return __builtin_bit_cast(u32, __builtin_convertvector(f, bf16x2_));
}
#define MFMA16(a, b, c) __builtin_amdgcn_mfma_f32_16x16x32_bf16(a, b, c, 0, 0, 0)

#define KV_H (20 * 40)
__global__ __launch_bounds__(256) void prep_kernel(
    const u16* __restrict__ wqkvA, const u16* __restrict__ wphi, const u16* __restrict__ woA,
    const u16* __restrict__ wqkvB, const u16* __restrict__ woB,
    u16* __restrict__ WtA, float* __restrict__ csA, u16* __restrict__ WtB, float* __restrict__ csB,
    u16* __restrict__ woAt, u16* __restrict__ woBt, float* __restrict__ kvacc) {
  __shared__ __attribute__((aligned(16))) float wpT[DH][20];
  __shared__ u16 tile[64][66];
  const int b = blockIdx.x, tid = threadIdx.x;
  if (b < 576) {
    const int kr = b % 3, sb = b / 3;
    const bool isA = sb < 96;
    const int bb = isA ? sb : sb - 96;
    const int t = bb / NH, h = bb % NH;
    const u16* W = isA ? wqkvA : wqkvB;
    const int k = kr * 192 + tid;
    u32 raw[9];
    if (tid < 192) {
      const u32* src = (const u32*)(W + (size_t)k * N3 + t * DM + h * DH);
#pragma unroll
      for (int d = 0; d < 9; ++d) raw[d] = src[d];
    }
    for (int i = tid; i < DH * DH; i += 256) {
      int d = i / DH, f = i - d * DH;
      wpT[f][d] = bf2f(wphi[h * DH * DH + i]);
    }
    __syncthreads();
    int nbase;
    if (isA) {
      if (t == 0) nbase = h * DH;
      else nbase = DM + (h / 4) * 144 + (t == 2 ? 72 : 0) + (h % 4) * DH;
    } else {
      nbase = t * DM + h * DH;
    }
    u16* Wt = isA ? WtA : WtB;
    const bool fold = isA && t < 2;
    if (tid < 192) {
      float in[DH];
#pragma unroll
      for (int d = 0; d < 9; ++d) {
        in[2 * d] = __uint_as_float(raw[d] << 16);
        in[2 * d + 1] = __uint_as_float(raw[d] & 0xffff0000u);
      }
#pragma unroll
      for (int f = 0; f < DH; ++f) {
        float a;
        if (fold) {
          float4 w0 = *(const float4*)&wpT[f][0], w1 = *(const float4*)&wpT[f][4], w2 = *(const float4*)&wpT[f][8];
          float4 w3 = *(const float4*)&wpT[f][12], w4 = *(const float4*)&wpT[f][16];
          a = in[0] * w0.x + in[1] * w0.y + in[2] * w0.z + in[3] * w0.w;
          a += in[4] * w1.x + in[5] * w1.y + in[6] * w1.z + in[7] * w1.w;
          a += in[8] * w2.x + in[9] * w2.y + in[10] * w2.z + in[11] * w2.w;
          a += in[12] * w3.x + in[13] * w3.y + in[14] * w3.z + in[15] * w3.w;
          a += in[16] * w4.x + in[17] * w4.y;
        } else {
          a = in[f];
        }
        Wt[(size_t)(nbase + f) * DM + k] = f2bf(a);
      }
    }
  } else if (b < 738) {
    const bool isA = b < 657;
    const int bb = isA ? b - 576 : b - 657;
    const int tr = bb / 9, tc = bb % 9;
    const u16* W = isA ? woA : woB;
    u16* Wt = isA ? woAt : woBt;
    for (int i = tid; i < 4096; i += 256) {
      int r = i / 64, c = i % 64;
      tile[r][c] = W[(size_t)(tr * 64 + r) * DM + tc * 64 + c];
    }
    __syncthreads();
    for (int i = tid; i < 4096; i += 256) {
      int r = i / 64, c = i % 64;
      Wt[(size_t)(tc * 64 + r) * DM + tr * 64 + c] = tile[c][r];
    }
  } else {
    float4* z = (float4*)kvacc;
    for (int i = tid + (b - 738) * 256; i < 2 * NH * KV_H / 4; i += 256 * 8) z[i] = make_float4(0.f, 0.f, 0.f, 0.f);
  }
}

#define BSTR 208
#define TSTR 152

static __device__ __forceinline__ void feat_map(const u16* src, u32* o  ) {
  float v[DH];
#pragma unroll
  for (int d = 0; d < 9; ++d) {
    u32 t = *(const u32*)(src + 2 * d);
    v[2 * d] = __uint_as_float(t << 16);
    v[2 * d + 1] = __uint_as_float(t & 0xffff0000u);
  }
  float mx = v[0], mn = v[0];
#pragma unroll
  for (int d = 1; d < DH; ++d) { mx = fmaxf(mx, v[d]); mn = fminf(mn, v[d]); }
  float ep[DH], en[DH], sp = 0.f, sn = 0.f;
#pragma unroll
  for (int d = 0; d < DH; ++d) {
    ep[d] = __expf(v[d] - mx); sp += ep[d];
    en[d] = __expf(mn - v[d]); sn += en[d];
  }
  float ip = 1.f / sp, in_ = 1.f / sn;
#pragma unroll
  for (int d = 0; d < 9; ++d) o[d] = cvtpk(ep[2 * d] * ip, ep[2 * d + 1] * ip);
#pragma unroll
  for (int d = 0; d < 9; ++d) o[9 + d] = cvtpk(en[2 * d] * in_, en[2 * d + 1] * in_);
}

#define QBSTR 80
template <bool IS_A>
__global__ __launch_bounds__(256, 2) void qkv_kernel(
    const u16* __restrict__ x, const u16* __restrict__ Wt, const float* __restrict__ colsum,
    u16* __restrict__ phiQ, u16* __restrict__ kvT,
    u16* __restrict__ qB, u16* __restrict__ kB, u16* __restrict__ vT) {
  __shared__ __attribute__((aligned(16))) unsigned char smem[2 * 144 * QBSTR * 2];
  u16* Bs = (u16*)smem;
  const int tid = threadIdx.x, lane = tid & 63, w = tid >> 6;
  const int xcd = blockIdx.x & 7, idx = blockIdx.x >> 3;
  const int mt = (xcd & 3) * 8 + (idx & 7), nt = (xcd >> 2) * 6 + (idx >> 3);
  const int row0 = mt * 64 + w * 16;
  const int lr = lane & 15, lg = lane >> 4;

  f32x4 acc[9];
#pragma unroll
  for (int j = 0; j < 9; ++j) acc[j] = (f32x4){0.f, 0.f, 0.f, 0.f};
  float s1 = 0.f, s2 = 0.f;

  const u16* xa = x + (size_t)(row0 + lr) * DM + lg * 8;
  const u16* Wg = Wt + (size_t)(nt * 144 + (tid >> 3)) * DM + (tid & 7) * 8;
  const int bso = (tid >> 3) * QBSTR + (tid & 7) * 8;
  bf16x8 rb[5], ra0, ra1, a0, a1;
  float wsum[5] = {0.f, 0.f, 0.f, 0.f, 0.f};
  auto gload = [&](int st) {
#pragma unroll
    for (int i = 0; i < 4; ++i) rb[i] = *(const bf16x8*)(Wg + (size_t)i * 32 * DM + st * 64);
    if (tid < 128) rb[4] = *(const bf16x8*)(Wg + (size_t)4 * 32 * DM + st * 64);
    ra0 = *(const bf16x8*)(xa + st * 64);
    ra1 = *(const bf16x8*)(xa + st * 64 + 32);
  };
  auto sstore = [&](int buf) {
#pragma unroll
    for (int i = 0; i < 4; ++i) *(bf16x8*)(Bs + buf * 144 * QBSTR + bso + i * 32 * QBSTR) = rb[i];
    if (tid < 128) *(bf16x8*)(Bs + buf * 144 * QBSTR + bso + 4 * 32 * QBSTR) = rb[4];
    typedef __attribute__((ext_vector_type(2))) __bf16 bf2v;
    const bf2v ones = {(__bf16)1.0f, (__bf16)1.0f};
#pragma unroll
    for (int i = 0; i < 5; ++i)
      if (i < 4 || tid < 128) {
        uint4 u = __builtin_bit_cast(uint4, rb[i]);
        wsum[i] = __builtin_amdgcn_fdot2_f32_bf16(__builtin_bit_cast(bf2v, u.x), ones, wsum[i], false);
        wsum[i] = __builtin_amdgcn_fdot2_f32_bf16(__builtin_bit_cast(bf2v, u.y), ones, wsum[i], false);
        wsum[i] = __builtin_amdgcn_fdot2_f32_bf16(__builtin_bit_cast(bf2v, u.z), ones, wsum[i], false);
        wsum[i] = __builtin_amdgcn_fdot2_f32_bf16(__builtin_bit_cast(bf2v, u.w), ones, wsum[i], false);
      }
  };
  gload(0);
  sstore(0);
  a0 = ra0; a1 = ra1;
  __syncthreads();
  for (int st = 0; st < 9; ++st) {
    const int buf = st & 1;
    if (st + 1 < 9) gload(st + 1);
    const u16* Bb = Bs + buf * 144 * QBSTR + lr * QBSTR + lg * 8;
#pragma unroll
    for (int j = 0; j < 9; ++j) {
      bf16x8 b0 = *(const bf16x8*)(Bb + j * 16 * QBSTR);
      bf16x8 b1 = *(const bf16x8*)(Bb + j * 16 * QBSTR + 32);
      acc[j] = MFMA16(b0, a0, acc[j]);
      acc[j] = MFMA16(b1, a1, acc[j]);
    }
#pragma unroll
    for (int e = 0; e < 8; ++e) {
      float f = bf2f((u16)a0[e]), g = bf2f((u16)a1[e]);
      s1 += f + g;
      s2 += f * f + g * g;
    }
    if (st + 1 < 9) { sstore(buf ^ 1); a0 = ra0; a1 = ra1; }
    __syncthreads();
  }
  u16* T = (u16*)smem;
  float* csl = (float*)(smem + 40960);
#pragma unroll
  for (int i = 0; i < 5; ++i) {
    float v = wsum[i];
    v += __shfl_xor(v, 1); v += __shfl_xor(v, 2); v += __shfl_xor(v, 4);
    if ((tid & 7) == 0 && (i < 4 || tid < 128)) csl[(tid >> 3) + i * 32] = v;
  }
  __syncthreads();
  {
    s1 += __shfl_xor(s1, 16); s1 += __shfl_xor(s1, 32);
    s2 += __shfl_xor(s2, 16); s2 += __shfl_xor(s2, 32);
    float m = s1 * (1.f / DM);
    float v = fmaxf(s2 * (1.f / DM) - m * m, 0.f);
    float rs = rsqrtf(v + 1e-5f);
#pragma unroll
    for (int j = 0; j < 9; ++j) {
      f32x4 cs = *(const f32x4*)(csl + j * 16 + lg * 4);
      float v0 = (acc[j][0] - m * cs[0]) * rs, v1 = (acc[j][1] - m * cs[1]) * rs;
      float v2 = (acc[j][2] - m * cs[2]) * rs, v3 = (acc[j][3] - m * cs[3]) * rs;
      *(uint2*)(T + (w * 16 + lr) * TSTR + j * 16 + lg * 4) = make_uint2(cvtpk(v0, v1), cvtpk(v2, v3));
    }
  }
  __syncthreads();
  if (IS_A) {
    if (nt < 4) {
#pragma unroll
      for (int it = 0; it < 2; ++it) {
        int task = tid + it * 256;
        int r = task >> 3, hh = task & 7;
        u32 o[18];
        feat_map(T + r * TSTR + hh * DH, o);
        u32* dst = (u32*)(phiQ + (size_t)(mt * 64 + r) * PQ_STRIDE + (nt * 8 + hh) * 40);
#pragma unroll
        for (int q = 0; q < 4; ++q)
          *(uint4*)(dst + q * 4) = make_uint4(o[q * 4], o[q * 4 + 1], o[q * 4 + 2], o[q * 4 + 3]);
        *(uint4*)(dst + 16) = make_uint4(o[16], o[17], 0u, 0u);
      }
    } else {
      u16* PK = (u16*)(smem + 64 * TSTR * 2);
      const int r = tid >> 2, hh = tid & 3;
      const u16* src = T + r * TSTR + hh * DH;
      u32 o[18], vv[9];
      feat_map(src, o);
#pragma unroll
      for (int d = 0; d < 9; ++d) vv[d] = *(const u32*)(src + 72 + 2 * d);
      __syncthreads();
      PK = (u16*)smem;
#pragma unroll
      for (int d = 0; d < 18; ++d) {
        PK[(hh * 54 + 2 * d) * 72 + r] = (u16)(o[d] & 0xffff);
        PK[(hh * 54 + 2 * d + 1) * 72 + r] = (u16)(o[d] >> 16);
      }
#pragma unroll
      for (int d = 0; d < 9; ++d) {
        PK[(hh * 54 + 36 + 2 * d) * 72 + r] = (u16)(vv[d] & 0xffff);
        PK[(hh * 54 + 36 + 2 * d + 1) * 72 + r] = (u16)(vv[d] >> 16);
      }
      __syncthreads();
      for (int i = tid; i < 4 * 54 * 8; i += 256) {
        int row = i >> 3, c8 = i & 7;
        int h4 = row / 54, rr = row - h4 * 54;
        uint4 val = *(const uint4*)(PK + row * 72 + c8 * 8);
        *(uint4*)(kvT + ((size_t)((nt - 4) * 4 + h4) * 54 + rr) * S_LEN + mt * 64 + c8 * 8) = val;
      }
    }
  } else {
    const int t = nt / 4, hb = (nt % 4) * 8;
    if (t < 2) {
      const float sc = (t == 0) ? 0.23570226039551584f * 1.4426950408889634f : 1.f;
      u16* dstb = (t == 0) ? qB : kB;
#pragma unroll
      for (int it = 0; it < 2; ++it) {
        int task = tid + it * 256;
        int hh = task >> 6, r = task & 63;
        const u16* src = T + r * TSTR + hh * DH;
        u32 o[9];
#pragma unroll
        for (int d = 0; d < 9; ++d) {
          u32 tt = *(const u32*)(src + 2 * d);
          o[d] = (t == 0) ? cvtpk(__uint_as_float(tt << 16) * sc, __uint_as_float(tt & 0xffff0000u) * sc) : tt;
        }
        u32* dst = (u32*)(dstb + ((size_t)(hb + hh) * S_LEN + mt * 64 + r) * 32);
        *(uint4*)(dst) = make_uint4(o[0], o[1], o[2], o[3]);
        *(uint4*)(dst + 4) = make_uint4(o[4], o[5], o[6], o[7]);
        *(uint4*)(dst + 8) = make_uint4(o[8], 0u, 0u, 0u);
        *(uint4*)(dst + 12) = make_uint4(0u, 0u, 0u, 0u);
      }
    } else {
      for (int i = tid; i < 144 * 8; i += 256) {
        int col = i >> 3, c8 = i & 7;
        int base = (c8 >> 1) * 16 + (c8 & 1) * 4;
        u32 o[4];
#pragma unroll
        for (int q = 0; q < 4; ++q) {
          int k0 = base + 8 * (q >> 1) + 2 * (q & 1);
          o[q] = (u32)T[k0 * TSTR + col] | ((u32)T[(k0 + 1) * TSTR + col] << 16);
        }
        *(uint4*)(vT + ((size_t)(hb * DH + col)) * S_LEN + mt * 64 + c8 * 8) = make_uint4(o[0], o[1], o[2], o[3]);
      }
    }
  }
}

__global__ __launch_bounds__(256) void kv_kernel(const u16* __restrict__ kvT, float* __restrict__ kvacc) {
  __shared__ float red[4][48][33];
  const int tid = threadIdx.x, lane = tid & 63, w = tid >> 6;
  const int lr = lane & 15, lg = lane >> 4;
  const int h = blockIdx.x >> 3, ch = blockIdx.x & 7;
  const u16* base = kvT + (size_t)h * 54 * S_LEN + ch * 256 + w * 64 + lg * 8;
  f32x4 c[3][2];
#pragma unroll
  for (int i = 0; i < 3; ++i)
#pragma unroll
    for (int j = 0; j < 2; ++j) c[i][j] = (f32x4){0.f, 0.f, 0.f, 0.f};
  const int f2 = min(32 + lr, 35);
  const bool v1ok = lr < 2;
  const short one = (short)0x3F80;
  bf16x8 cst = (lr == 2) ? (bf16x8){one, one, one, one, one, one, one, one} : (bf16x8){0, 0, 0, 0, 0, 0, 0, 0};
#pragma unroll
  for (int s = 0; s < 2; ++s) {
    bf16x8 a0 = *(const bf16x8*)(base + (size_t)lr * S_LEN + s * 32);
    bf16x8 a1 = *(const bf16x8*)(base + (size_t)(16 + lr) * S_LEN + s * 32);
    bf16x8 a2 = *(const bf16x8*)(base + (size_t)f2 * S_LEN + s * 32);
    bf16x8 b0 = *(const bf16x8*)(base + (size_t)(36 + lr) * S_LEN + s * 32);
    bf16x8 b1 = *(const bf16x8*)(base + (size_t)(36 + 16 + (v1ok ? lr : 0)) * S_LEN + s * 32);
    if (!v1ok) b1 = cst;
    c[0][0] = MFMA16(a0, b0, c[0][0]); c[0][1] = MFMA16(a0, b1, c[0][1]);
    c[1][0] = MFMA16(a1, b0, c[1][0]); c[1][1] = MFMA16(a1, b1, c[1][1]);
    c[2][0] = MFMA16(a2, b0, c[2][0]); c[2][1] = MFMA16(a2, b1, c[2][1]);
  }
#pragma unroll
  for (int i = 0; i < 3; ++i)
#pragma unroll
    for (int j = 0; j < 2; ++j)
#pragma unroll
      for (int r = 0; r < 4; ++r) red[w][i * 16 + lg * 4 + r][j * 16 + lr] = c[i][j][r];
  __syncthreads();
  for (int i = tid; i < 760; i += 256) {
    int d = i / 40, f = i - d * 40;
    if (f < 36) atomicAdd(&kvacc[h * 800 + i], red[0][f][d] + red[1][f][d] + red[2][f][d] + red[3][f][d]);
  }
}

__global__ __launch_bounds__(256) void o_kernel(const u16* __restrict__ phiQ, const float* __restrict__ kvacc,
                                                u16* __restrict__ oB) {
  __shared__ __attribute__((aligned(16))) u16 KVT[8 * 800 + 64];
  const int tid = threadIdx.x, lane = tid & 63, w = tid >> 6;
  const int lr = lane & 15, lg = lane >> 4;
  const int mt = blockIdx.x >> 2, hg = blockIdx.x & 3;
  const int row0 = mt * 32;
  bf16x8 pa[2][2][2];
#pragma unroll
  for (int hh = 0; hh < 2; ++hh)
#pragma unroll
    for (int i = 0; i < 2; ++i) {
      const u16* ap = phiQ + (size_t)(row0 + i * 16 + lr) * PQ_STRIDE + (hg * 8 + w * 2 + hh) * 40;
      pa[hh][i][0] = *(const bf16x8*)(ap + lg * 8);
      pa[hh][i][1] = *(const bf16x8*)(ap + 32);
    }
  {
    const float4* k4 = (const float4*)(kvacc + hg * 6400);
    float4 t[6], t6 = make_float4(0.f, 0.f, 0.f, 0.f);
#pragma unroll
    for (int i = 0; i < 6; ++i) t[i] = k4[tid + i * 256];
    if (tid < 64) t6 = k4[tid + 1536];
#pragma unroll
    for (int i = 0; i < 6; ++i) *(uint2*)(KVT + (tid + i * 256) * 4) = make_uint2(cvtpk(t[i].x, t[i].y), cvtpk(t[i].z, t[i].w));
    if (tid < 64) *(uint2*)(KVT + (tid + 1536) * 4) = make_uint2(cvtpk(t6.x, t6.y), cvtpk(t6.z, t6.w));
  }
  __syncthreads();
#pragma unroll
  for (int hh = 0; hh < 2; ++hh) {
    const int hl = w * 2 + hh, h = hg * 8 + hl;
    bf16x8 bb[2][2];
#pragma unroll
    for (int j = 0; j < 2; ++j) {
      int d = j * 16 + lr;
      int drow = d < 19 ? d : 19;
      bb[j][0] = *(const bf16x8*)(KVT + (hl * 20 + drow) * 40 + lg * 8);
      bb[j][1] = *(const bf16x8*)(KVT + (hl * 20 + (lg == 0 ? drow : 19)) * 40 + 32);
    }
#pragma unroll
    for (int i = 0; i < 2; ++i) {
      f32x4 c0 = {0.f, 0.f, 0.f, 0.f}, c1 = {0.f, 0.f, 0.f, 0.f};
      c0 = MFMA16(pa[hh][i][0], bb[0][0], c0);
      c0 = MFMA16(pa[hh][i][1], bb[0][1], c0);
      c1 = MFMA16(pa[hh][i][0], bb[1][0], c1);
      c1 = MFMA16(pa[hh][i][1], bb[1][1], c1);
#pragma unroll
      for (int r = 0; r < 4; ++r) {
        float den = __shfl(c1[r], (lane & 48) + 2);
        den = bf2f(f2bf(bf2f(f2bf(den)) + 1e-6f));
        float inv = __builtin_amdgcn_rcpf(den);
        u16* dst = oB + (size_t)(row0 + i * 16 + lg * 4 + r) * DM + h * DH;
        dst[lr] = f2bf(bf2f(f2bf(c0[r])) * inv);
        if (lr < 2) dst[16 + lr] = f2bf(bf2f(f2bf(c1[r])) * inv);
      }
    }
  }
}

#define OSTR 584
__global__ __launch_bounds__(256, 1) void out_kernel(
    const u16* __restrict__ oB, const u16* __restrict__ Wot, const u16* __restrict__ xin,
    u16* __restrict__ xout) {
  __shared__ __attribute__((aligned(16))) u16 O[64 * OSTR];
  const int tid = threadIdx.x, lane = tid & 63, w = tid >> 6;
  const int xcd = blockIdx.x & 7, idx = blockIdx.x >> 3;
  const int mt = xcd * 4 + (idx & 3), nt = idx >> 2;
  const int lr = lane & 15, lg = lane >> 4;
  const int row0 = mt * 64, rw = (w & 1) * 32;
  const int col0 = nt * 96 + (w >> 1) * 48;
  bf16x8 t[18];
#pragma unroll
  for (int i = 0; i < 18; ++i) {
    int c = tid + i * 256, r = c / 72, cc = c - r * 72;
    t[i] = *(const bf16x8*)(oB + (size_t)(row0 + r) * DM + cc * 8);
  }
  bf16x8 wb[3][18];
  {
    const u16* wp = Wot + (size_t)(col0 + lr) * DM + lg * 8;
#pragma unroll
    for (int j = 0; j < 3; ++j)
#pragma unroll
      for (int s = 0; s < 18; ++s) wb[j][s] = *(const bf16x8*)(wp + (size_t)j * 16 * DM + s * 32);
  }
#pragma unroll
  for (int i = 0; i < 18; ++i) {
    int c = tid + i * 256, r = c / 72, cc = c - r * 72;
    *(bf16x8*)(O + r * OSTR + cc * 8) = t[i];
  }
  u16 xr[2][3][4];
#pragma unroll
  for (int i = 0; i < 2; ++i)
#pragma unroll
    for (int j = 0; j < 3; ++j)
#pragma unroll
      for (int r = 0; r < 4; ++r)
        xr[i][j][r] = xin[(size_t)(row0 + rw + i * 16 + lg * 4 + r) * DM + col0 + j * 16 + lr];
  __syncthreads();
  f32x4 acc[2][3];
#pragma unroll
  for (int i = 0; i < 2; ++i)
#pragma unroll
    for (int j = 0; j < 3; ++j) acc[i][j] = (f32x4){0.f, 0.f, 0.f, 0.f};
#pragma unroll
  for (int s = 0; s < 18; ++s) {
    bf16x8 a0 = *(const bf16x8*)(O + (rw + lr) * OSTR + s * 32 + lg * 8);
    bf16x8 a1 = *(const bf16x8*)(O + (rw + 16 + lr) * OSTR + s * 32 + lg * 8);
#pragma unroll
    for (int j = 0; j < 3; ++j) {
      acc[0][j] = MFMA16(a0, wb[j][s], acc[0][j]);
      acc[1][j] = MFMA16(a1, wb[j][s], acc[1][j]);
    }
  }
#pragma unroll
  for (int i = 0; i < 2; ++i)
#pragma unroll
    for (int j = 0; j < 3; ++j)
#pragma unroll
      for (int r = 0; r < 4; ++r) {
        size_t idx2 = (size_t)(row0 + rw + i * 16 + lg * 4 + r) * DM + col0 + j * 16 + lr;
        float y = bf2f(f2bf(acc[i][j][r]));
        xout[idx2] = f2bf(bf2f(xr[i][j][r]) + y);
      }
}

typedef __attribute__((ext_vector_type(16))) float f32x16;
#define MFMA32(a, b, c) __builtin_amdgcn_mfma_f32_32x32x16_bf16(a, b, c, 0, 0, 0)
#define KSTR 40
#define VSTR 72
#define EXP2(x) __builtin_amdgcn_exp2f(x)
#define ATHR 8.0f
#define NKS 2
#define NST (32 / NKS)
__global__ __launch_bounds__(256 * NKS, 4) void attn_kernel(
    const u16* __restrict__ qB, const u16* __restrict__ kB, const u16* __restrict__ vT,
    u16* __restrict__ oB) {
  __shared__ __attribute__((aligned(16))) u16 Ks[3][NKS][64 * KSTR];
  __shared__ __attribute__((aligned(16))) u16 Vs[3][NKS][32 * VSTR];
  const int tid = threadIdx.x, lane = tid & 63, w = tid >> 6;
  const int l31 = lane & 31, hh = lane >> 5;
  const int qg = w & 3, ks = w >> 2;
  const int qt = blockIdx.x, h = blockIdx.y;
  const int q0 = qt * 128 + qg * 32;
  const int lkh = tid >> 8, lkr = (tid >> 2) & 63, lkc = tid & 3;
  const u16* Kg = kB + (size_t)h * S_LEN * 32 + (size_t)(lkh * (S_LEN / NKS) + lkr) * 32 + lkc * 8;
  const int kso = lkh * 64 * KSTR + lkr * KSTR + lkc * 8;
  const int lvh = tid / 144, lvr = (tid % 144) >> 3, lvc = tid & 7;
  const u16* Vg = vT + (size_t)h * DH * S_LEN + (size_t)lvr * S_LEN + lvh * (S_LEN / NKS) + lvc * 8;
  const int vso = lvh * 32 * VSTR + lvr * VSTR + lvc * 8;
  const bool vl = tid < 144 * NKS;

  bf16x8 rk, rv, rk1, rv1, rk2, rv2;
  rk1 = *(const bf16x8*)(Kg);
  rk2 = *(const bf16x8*)(Kg + 64 * 32);
  rk = *(const bf16x8*)(Kg + 2 * 64 * 32);
  if (vl) {
    rv1 = *(const bf16x8*)(Vg);
    rv2 = *(const bf16x8*)(Vg + 64);
    rv = *(const bf16x8*)(Vg + 128);
  }
  bf16x8 qf[2];
#pragma unroll
  for (int s = 0; s < 2; ++s)
    qf[s] = *(const bf16x8*)(qB + ((size_t)h * S_LEN + q0 + l31) * 32 + s * 16 + hh * 8);
  for (int i = tid; i < 3 * NKS * 14 * 32; i += 256 * NKS) {
    int b = i / (14 * 32), rem = i - b * (14 * 32);
    int d = 18 + (rem >> 5);
    *(u32*)(&Vs[0][0][0] + b * 32 * VSTR + d * VSTR + (rem & 31) * 2) = (d == 18) ? 0x3F803F80u : 0u;
  }
  *(bf16x8*)(&Ks[0][0][kso]) = rk1;
  *(bf16x8*)(&Ks[1][0][kso]) = rk2;
  if (vl) {
    *(bf16x8*)(&Vs[0][0][vso]) = rv1;
    *(bf16x8*)(&Vs[1][0][vso]) = rv2;
  }
  auto gload = [&](int st) {
    rk = *(const bf16x8*)(Kg + (size_t)st * 64 * 32);
    if (vl) rv = *(const bf16x8*)(Vg + st * 64);
  };
  auto sstore = [&](int buf) {
    *(bf16x8*)(&Ks[buf][0][kso]) = rk;
    if (vl) *(bf16x8*)(&Vs[buf][0][vso]) = rv;
  };
  f32x16 o, negm;
#pragma unroll
  for (int r = 0; r < 16; ++r) { o[r] = 0.f; negm[r] = 0.f; }
  float mref = 0.f;
  __syncthreads();
  auto qk = [&](int buf, int half, f32x16& a0) {
    const u16* Kb = &Ks[buf][ks][half * 32 * KSTR];
    bf16x8 k00 = *(const bf16x8*)(Kb + l31 * KSTR + hh * 8);
    bf16x8 k01 = *(const bf16x8*)(Kb + l31 * KSTR + 16 + hh * 8);
    a0 = MFMA32(k00, qf[0], negm);
    a0 = MFMA32(k01, qf[1], a0);
  };
  auto smpv = [&](f32x16& sc, f32x16& so, const u16* Vb, bool first) {
    float m0 = fmaxf(fmaxf(sc[0], sc[1]), sc[2]);
    float m1 = fmaxf(fmaxf(sc[3], sc[4]), sc[5]);
    float m2 = fmaxf(fmaxf(sc[6], sc[7]), sc[8]);
    float m3 = fmaxf(fmaxf(sc[9], sc[10]), sc[11]);
    m0 = fmaxf(fmaxf(m0, sc[12]), sc[13]);
    m1 = fmaxf(fmaxf(m1, sc[14]), sc[15]);
    float mx = fmaxf(fmaxf(m0, m1), fmaxf(m2, m3));
    {
      auto rr = __builtin_amdgcn_permlane32_swap(__float_as_uint(mx), __float_as_uint(mx), false, false);
      mx = fmaxf(__uint_as_float(rr[0]), __uint_as_float(rr[1]));
    }
    if (first || __builtin_amdgcn_ballot_w64(mx > ATHR) != 0) {
      float delta = first ? mx : fmaxf(mx, 0.f);
      float alpha = first ? 1.f : EXP2(-delta);
      mref += delta;
#pragma unroll
      for (int r = 0; r < 16; ++r) {
        negm[r] = -mref;
        sc[r] -= delta;
        so[r] -= delta;
      }
#pragma unroll
      for (int r = 0; r < 12; ++r) o[r] *= alpha;
    }
    u32 p[8];
#pragma unroll
    for (int r = 0; r < 8; ++r) p[r] = cvtpk(EXP2(sc[2 * r]), EXP2(sc[2 * r + 1]));
#pragma unroll
    for (int u = 0; u < 2; ++u) {
      bf16x8 vf = *(const bf16x8*)(Vb + l31 * VSTR + u * 16 + hh * 8);
      bf16x8 pf = __builtin_bit_cast(bf16x8, *(uint4*)(p + 4 * u));
      o = MFMA32(vf, pf, o);
    }
  };
  f32x16 sa, sb;
  qk(0, 0, sa);
  int bc = 0;
  for (int st = 0; st < NST; ++st) {
    const int bn = (bc == 2) ? 0 : bc + 1;
    const int bs = (bn == 2) ? 0 : bn + 1;
    const u16* Vb = &Vs[bc][ks][0];
    qk(bc, 1, sb);
    smpv(sa, sb, Vb, st == 0);
    if (st + 1 < NST) qk(bn, 0, sa);
    smpv(sb, sa, Vb + 32, false);
    if (st + 2 < NST) sstore(bs);
    if (st + 3 < NST) gload(st + 3);
    __syncthreads();
    bc = bn;
  }
  float* cb = (float*)&Ks[0][0][0];
  if (ks != 0) {
    float* c = cb + ((ks - 1) * 4 + qg) * 13 * 64;
#pragma unroll
    for (int r = 0; r < 12; ++r) c[r * 64 + lane] = o[r];
    c[12 * 64 + lane] = mref;
  }
  __syncthreads();
  if (ks == 0) {
    float mn = mref;
    float mo[NKS - 1];
#pragma unroll
    for (int k = 0; k < NKS - 1; ++k) { mo[k] = cb[((k * 4 + qg) * 13 + 12) * 64 + lane]; mn = fmaxf(mn, mo[k]); }
    float a0 = EXP2(mref - mn);
#pragma unroll
    for (int r = 0; r < 12; ++r) o[r] *= a0;
#pragma unroll
    for (int k = 0; k < NKS - 1; ++k) {
      float a1 = EXP2(mo[k] - mn);
#pragma unroll
      for (int r = 0; r < 12; ++r) o[r] += cb[((k * 4 + qg) * 13 + r) * 64 + lane] * a1;
    }
    float l = __shfl(o[10], l31);
    float inv = 1.f / l;
    u16* dst = oB + (size_t)(q0 + l31) * DM + h * DH;
    *(uint2*)(dst + 4 * hh) = make_uint2(cvtpk(o[0] * inv, o[1] * inv), cvtpk(o[2] * inv, o[3] * inv));
    *(uint2*)(dst + 8 + 4 * hh) = make_uint2(cvtpk(o[4] * inv, o[5] * inv), cvtpk(o[6] * inv, o[7] * inv));
    if (hh == 0) *(u32*)(dst + 16) = cvtpk(o[8] * inv, o[9] * inv);
  }
}

extern "C" void kernel_launch(void* const* d_in, const int* in_sizes, int n_in,
                              void* d_out, int out_size, void* d_ws, size_t ws_size,
                              hipStream_t stream) {
  const u16* x0 = (const u16*)d_in[0];
  const u16* wqkvA = (const u16*)d_in[1];
  const u16* wphi = (const u16*)d_in[2];
  const u16* woA = (const u16*)d_in[3];
  const u16* wqkvB = (const u16*)d_in[5];
  const u16* woB = (const u16*)d_in[6];
  u16* out = (u16*)d_out;

  char* ws = (char*)d_ws;
  size_t off = 0;
  auto alloc = [&](size_t bytes) { void* p = ws + off; off += (bytes + 255) & ~(size_t)255; return p; };
  u16* WtA = (u16*)alloc((size_t)N3 * DM * 2);
  u16* WtB = (u16*)alloc((size_t)N3 * DM * 2);
  float* csA = (float*)alloc(N3 * 4);
  float* csB = (float*)alloc(N3 * 4);
  u16* woAt = (u16*)alloc((size_t)DM * DM * 2);
  u16* woBt = (u16*)alloc((size_t)DM * DM * 2);
  float* kvacc = (float*)alloc(2 * NH * KV_H * 4);
  u16* kvT = (u16*)alloc((size_t)NH * 54 * S_LEN * 2);
  u16* kvb = (u16*)alloc(NH * 800 * 2);
  u16* phiQ = (u16*)alloc((size_t)S_LEN * PQ_STRIDE * 2 + 4096);
  u16* x1 = (u16*)alloc((size_t)S_LEN * DM * 2);
  u16* x2 = (u16*)alloc((size_t)S_LEN * DM * 2);
  u16* x3 = (u16*)alloc((size_t)S_LEN * DM * 2);
  u16* qB = (u16*)alloc((size_t)NH * S_LEN * 32 * 2);
  u16* kB = (u16*)alloc((size_t)NH * S_LEN * 32 * 2);
  u16* vT = (u16*)alloc((size_t)NH * DH * S_LEN * 2);
  u16* oB = (u16*)alloc((size_t)S_LEN * DM * 2);

  prep_kernel<<<746, 256, 0, stream>>>(wqkvA, wphi, woA, wqkvB, woB, WtA, csA, WtB, csB, woAt, woBt, kvacc);

  const u16* xin[4] = {x0, x1, x2, x3};
  u16* xo[4] = {x1, x2, x3, out};
  for (int layer = 0; layer < 2; ++layer) {
    qkv_kernel<true><<<384, 256, 0, stream>>>(xin[2 * layer], WtA, csA, phiQ, kvT, nullptr, nullptr, nullptr);
    kv_kernel<<<256, 256, 0, stream>>>(kvT, kvacc + layer * NH * KV_H);
    o_kernel<<<256, 256, 0, stream>>>(phiQ, kvacc + layer * NH * KV_H, oB);
    out_kernel<<<192, 256, 0, stream>>>(oB, woAt, xin[2 * layer], xo[2 * layer]);
    qkv_kernel<false><<<384, 256, 0, stream>>>(xin[2 * layer + 1], WtB, csB, nullptr, nullptr, qB, kB, vT);
    attn_kernel<<<dim3(16, 32), 256 * NKS, 0, stream>>>(qB, kB, vT, oB);
    out_kernel<<<192, 256, 0, stream>>>(oB, woBt, xin[2 * layer + 1], xo[2 * layer + 1]);
  }
}
```
